```python
import torch

def _softmax(x, axis=-1):
    return torch.softmax(x, dim=axis)
def _gelu(x):
    return torch.nn.functional.gelu(x, approximate="tanh")
def _silu(x):
    return torch.nn.functional.silu(x)
def _sigmoid(x):
    return torch.sigmoid(x)
def _layernorm(x, axis=-1, eps=1e-5):
    m = x.mean(dim=axis, keepdim=True)
    v = x.var(dim=axis, keepdim=True, unbiased=False)
    return (x - m) / torch.sqrt(v + eps)
def _scatter_add(tgt, idx, src, axis=0):
    return tgt.clone().index_add_(axis, idx, src)
def _segment_sum(src, seg, n):
    out = torch.zeros((n,) + src.shape[1:], dtype=src.dtype)
    return out.index_add_(0, seg, src)
def _pad(x, pw):
    flat = tuple(v for pair in reversed(pw) for v in pair)
    return torch.nn.functional.pad(x, flat)

kernel_name = 'concat_heads__linear_attn_block__grouped_kv_mqa__prompt'

def setup_inputs():

    _w = (lambda a: (lambda t: t.to(torch.bfloat16) if t.is_floating_point() else t)(torch.as_tensor(a)))
    def _A_setup():
        B, S, D = 1, 2048, 1024
        return {
            "x": _w((lambda *s: torch.randn(*s, dtype=torch.bfloat16) * 0.05)(B, S, D)),
            "wqkv": _w((lambda *s: torch.randn(*s, dtype=torch.bfloat16) * 0.8)(D, 3 * D)),
            "wo": _w((lambda *s: torch.randn(*s, dtype=torch.bfloat16) * 0.05)(D, D)),
        }
    _A_in = _A_setup()

    def _B_setup():
        B, S, D, H = 1, 2048, 1024, 32
        Dh = D // H
        return {
            "x": _w((lambda *s: torch.randn(*s, dtype=torch.bfloat16) * 0.05)(B, S, D)),
            "wq": _w((lambda *s: torch.randn(*s, dtype=torch.bfloat16) * 0.8)(D, H * Dh)),
            "wk": _w((lambda *s: torch.randn(*s, dtype=torch.bfloat16) * 0.8)(D, Dh)),
            "wv": _w((lambda *s: torch.randn(*s, dtype=torch.bfloat16) * 0.05)(D, Dh)),
            "wo": _w((lambda *s: torch.randn(*s, dtype=torch.bfloat16) * 0.05)(H * Dh, D)),
        }
    _B_in = _B_setup()

    return {
        "A_x": _A_in["x"],
        "A_wqkv": _A_in["wqkv"],
        "A_wo": _A_in["wo"],
        "B_x": _B_in["x"],
        "B_wq": _B_in["wq"],
        "B_wk": _B_in["wk"],
        "B_wv": _B_in["wv"],
        "B_wo": _B_in["wo"],
    }

def reference(A_x, A_wqkv, A_wo, B_x, B_wq, B_wk, B_wv, B_wo):
    def _A_ref(x, wqkv, wo):
        B, S, D = x.shape
        H = 32; Dh = D // H
        qkv = (lambda x, *d: x.permute(*d))((x @ wqkv).reshape(B, S, 3, H, Dh), 2, 0, 3, 1, 4)
        q, k, v = qkv[0], qkv[1], qkv[2]
        phi = lambda t: torch.where(t > 0, t + 1, torch.exp(t)) + 1e-6
        pq, pk = phi(q), phi(k)
        kv = torch.einsum("bhsd,bhse->bhde", pk, v)
        z = torch.einsum("bhsd->bhd", pk)
        num = torch.einsum("bhqd,bhde->bhqe", pq, kv)
        den = torch.einsum("bhqd,bhd->bhq", pq, z)[..., None] + 1e-6
        o = num / den
        return (lambda x, *d: x.permute(*d))(o, 0, 2, 1, 3).reshape(B, S, D) @ wo

    def _B_ref(x, wq, wk, wv, wo):
        B, S, D = x.shape
        H = 32
        Dh = D // H
        q = (lambda x, *d: x.permute(*d))((x @ wq).reshape(B, S, H, Dh), 0, 2, 1, 3)
        k = x @ wk
        v = x @ wv
        scores = torch.einsum("bhqd,bkd->bhqk", q, k) * (Dh ** -0.5)
        p = _softmax(scores, axis=-1)
        o = torch.einsum("bhqk,bkd->bhqd", p, v)
        return (lambda x, *d: x.permute(*d))(o, 0, 2, 1, 3).reshape(B, S, H * Dh) @ wo

    a = _A_ref(A_x, A_wqkv, A_wo)
    b = _B_ref(B_x, B_wq, B_wk, B_wv, B_wo)
    return torch.concatenate([a, b], axis=-1)
```

```cpp
#include <hip/hip_runtime.h>
#include <hip/hip_bf16.h>
typedef __hip_bfloat16 bf16;
__device__ inline float b2f(bf16 x) { return __bfloat162float(x); }
template<typename TO>
__global__ void gemm(const bf16* A, const bf16* B, TO* C, int M, int N, int K, int ldc) {
  int n = blockIdx.x * 64 + (threadIdx.x & 63), m = blockIdx.y * 4 + (threadIdx.x >> 6);
  if (n >= N) return;
  float s = 0;
  for (int k = 0; k < K; k++) s += b2f(A[m * K + k]) * b2f(B[k * N + n]);
  C[m * ldc + n] = (TO)s;
}
__device__ inline float phi(float t) { return (t > 0 ? t + 1 : __expf(t)) + 1e-6f; }
__global__ void kvz(const bf16* qkv, float* kv, float* z) {
  int h = blockIdx.x, d = threadIdx.x >> 5, e = threadIdx.x & 31;
  float s = 0, zz = 0;
  for (int t = 0; t < 2048; t++) { float pk = phi(b2f(qkv[t * 3072 + 1024 + h * 32 + d])); s += pk * b2f(qkv[t * 3072 + 2048 + h * 32 + e]); zz += pk; }
  kv[h * 1024 + d * 32 + e] = s; if (e == 0) z[h * 32 + d] = zz;
}
__global__ void lino(const bf16* qkv, const float* kv, const float* z, bf16* o) {
  int t = blockIdx.x, h = threadIdx.x >> 5, e = threadIdx.x & 31;
  float num = 0, den = 0;
  for (int d = 0; d < 32; d++) { float pq = phi(b2f(qkv[t * 3072 + h * 32 + d])); num += pq * kv[h * 1024 + d * 32 + e]; den += pq * z[h * 32 + d]; }
  o[t * 1024 + h * 32 + e] = (bf16)(num / (den + 1e-6f));
}
__global__ void attn(const bf16* q, const bf16* k, const bf16* v, bf16* o) {
  int i = blockIdx.x * 64 + threadIdx.x;
  float qq[32], acc[32]; for (int d = 0; d < 32; d++) { qq[d] = b2f(q[i * 32 + d]) * 0.17677669529f; acc[d] = 0; }
  float m = -1e30f, l = 0;
  for (int t = 0; t < 2048; t++) {
    float s = 0; for (int d = 0; d < 32; d++) s += qq[d] * b2f(k[t * 32 + d]);
    float mn = fmaxf(m, s), a = __expf(m - mn), p = __expf(s - mn); l = l * a + p; m = mn;
    for (int d = 0; d < 32; d++) acc[d] = acc[d] * a + p * b2f(v[t * 32 + d]);
  }
  for (int d = 0; d < 32; d++) o[i * 32 + d] = (bf16)(acc[d] / l);
}
extern "C" void kernel_launch(void* const* d_in, const int* in_sizes, int n_in, void* d_out, int out_size, void* d_ws, size_t ws_size, hipStream_t stream) {
  const bf16 *Ax = (const bf16*)d_in[0], *Aw = (const bf16*)d_in[1], *Awo = (const bf16*)d_in[2], *Bx = (const bf16*)d_in[3], *Bwq = (const bf16*)d_in[4], *Bwk = (const bf16*)d_in[5], *Bwv = (const bf16*)d_in[6], *Bwo = (const bf16*)d_in[7];
  bf16* out = (bf16*)d_out; char* ws = (char*)d_ws;
  bf16* qkv = (bf16*)ws; bf16* oA = (bf16*)(ws + (16 << 20)); float* kv = (float*)(ws + (24 << 20)); float* z = (float*)(ws + (25 << 20));
  bf16* q = (bf16*)(ws + (32 << 20)); bf16* k = (bf16*)(ws + (40 << 20)); bf16* v = (bf16*)(ws + (41 << 20)); bf16* oB = (bf16*)(ws + (48 << 20));
  gemm<bf16><<<dim3(48, 512), 256, 0, stream>>>(Ax, Aw, qkv, 2048, 3072, 1024, 3072);
  kvz<<<32, 1024, 0, stream>>>(qkv, kv, z);
  lino<<<2048, 1024, 0, stream>>>(qkv, kv, z, oA);
  gemm<bf16><<<dim3(16, 512), 256, 0, stream>>>(oA, Awo, out, 2048, 1024, 1024, 2048);
  gemm<bf16><<<dim3(16, 512), 256, 0, stream>>>(Bx, Bwq, q, 2048, 1024, 1024, 1024);
  gemm<bf16><<<dim3(1, 512), 256, 0, stream>>>(Bx, Bwk, k, 2048, 32, 1024, 32);
  gemm<bf16><<<dim3(1, 512), 256, 0, stream>>>(Bx, Bwv, v, 2048, 32, 1024, 32);
  attn<<<1024, 64, 0, stream>>>(q, k, v, oB);
  gemm<bf16><<<dim3(16, 512), 256, 0, stream>>>(oB, Bwo, out + 1024, 2048, 1024, 1024, 2048);
}
```

```cpp
#include <hip/hip_runtime.h>
#include <hip/hip_bf16.h>
#include <stdint.h>
#define K0A ((lc * 2 + lr * 4) & 15)
#define K0B ((lc * 2 + lr * 4) & 15)
#define ATTN_H "attn_asm.h"

typedef __bf16 bf16x8 __attribute__((ext_vector_type(8)));
typedef __bf16 bf16x2 __attribute__((ext_vector_type(2)));
typedef short s16x4 __attribute__((ext_vector_type(4)));
typedef float f32x16 __attribute__((ext_vector_type(16)));
typedef float f32x4 __attribute__((ext_vector_type(4)));
typedef float f32x2 __attribute__((ext_vector_type(2)));
typedef unsigned u32x4 __attribute__((ext_vector_type(4)));
typedef unsigned u32x2 __attribute__((ext_vector_type(2)));
typedef __attribute__((address_space(3))) char* lds_ptr;
#define DI __device__ __forceinline__
#define SBAR() __builtin_amdgcn_sched_barrier(0)
#define PIN(x) asm volatile("" : "+v"(x))
#define MFMA32(a, b, c) __builtin_amdgcn_mfma_f32_32x32x16_bf16(a, b, c, 0, 0, 0)
#define MFMA16(a, b, c) __builtin_amdgcn_mfma_f32_16x16x32_bf16(a, b, c, 0, 0, 0)

constexpr size_t WS_QB = 0, WS_QA = 4u << 20, WS_OA = 8u << 20, WS_OB = 12u << 20, WS_KVB = 16u << 20, WS_KVP = 17u << 20, WS_ZP = 18u << 20;
constexpr float QSCALE = 0.17677669529663687f * 1.4426950408889634f;
constexpr float LOG2E = 1.4426950408889634f;

#define STAMP(k, i)
DI unsigned rfl(unsigned x) { return __builtin_amdgcn_readfirstlane(x); }
DI unsigned cvtpk(float lo, float hi) { f32x2 v = {lo, hi}; return __builtin_bit_cast(unsigned, __builtin_convertvector(v, bf16x2)); }
DI bf16x8 pack8(const f32x16& v, int o) { u32x4 w = {cvtpk(v[o], v[o + 1]), cvtpk(v[o + 2], v[o + 3]), cvtpk(v[o + 4], v[o + 5]), cvtpk(v[o + 6], v[o + 7])}; return __builtin_bit_cast(bf16x8, w); }
DI __amdgpu_buffer_rsrc_t mk_srd(const void* p, unsigned bytes) { return __builtin_amdgcn_make_buffer_rsrc((void*)p, 0, bytes, 0x00020000); }
DI void dma16(unsigned ldsaddr, unsigned voff, __amdgpu_buffer_rsrc_t srd, unsigned soff) {
  asm volatile("s_mov_b32 m0, %0\n\ts_nop 0\n\tbuffer_load_dwordx4 %1, %2, %3 offen lds" ::"s"(ldsaddr), "v"(voff), "s"(srd), "s"(soff) : "memory");
}
#define WAITV(n) asm volatile("s_waitcnt vmcnt(" #n ")" ::: "memory")
#define BARRIER() asm volatile("s_waitcnt lgkmcnt(0)\n\ts_barrier" ::: "memory")
DI bf16x8 lds128(lds_ptr p) { return *(const __attribute__((address_space(3))) bf16x8*)p; }
DI bf16x8 ldstr(lds_ptr p0, lds_ptr p1) {
  typedef short v4i16 __attribute__((ext_vector_type(4)));
  v4i16 a = __builtin_amdgcn_ds_read_tr16_b64_v4i16((__attribute__((address_space(3))) v4i16*)p0);
  v4i16 b = __builtin_amdgcn_ds_read_tr16_b64_v4i16((__attribute__((address_space(3))) v4i16*)p1);
  return __builtin_bit_cast(bf16x8, __builtin_shufflevector(a, b, 0, 1, 2, 3, 4, 5, 6, 7));
}
DI float phi(float t) { float e = __builtin_amdgcn_fmed3f(__builtin_amdgcn_exp2f(t * LOG2E), 0.f, 1.f); return __builtin_fmaxf(t, 0.f) + (e + 1e-6f); }

constexpr int K1_SLOT = 49152, K1_XB = 32768, K1_KVOFF = 3 * K1_SLOT, K1_LDS = K1_KVOFF + 2 * 8192;

template <bool SWAP, int EXTRA>
DI void k1_mainloop(lds_ptr lds, int wr, int wc, int lane, int xrow, f32x16 (&acc)[4][2], f32x16& accx) {
  const int r = lane & 31, h = lane >> 5, g = (r >> 1) & 7;
  const int q = (lane & 15) >> 2, p = lane & 3, blk = (lane >> 4) & 1;
  unsigned xa[4];
#pragma unroll
  for (int s = 0; s < 4; ++s) xa[s] = (wr * 128 + r) * 128 + (((2 * s + h) ^ g) << 4);
  const unsigned wa = K1_XB + (8 * h + q) * 128 + ((wc ^ (q >> 1)) << 6) + blk * 32 + p * 8;
  const unsigned ea = K1_KVOFF + (EXTRA == 2 ? 4096 : 0) + (8 * h + q) * 64 + blk * 32 + p * 8;
  const int xe = (xrow - wr * 128) * 128;
  bf16x8 a[2][4], b[2][2], ae[2], be[2];
  unsigned slot = 0, eslot = 0;
#define K1_RD(F, S) do { lds_ptr ws_ = lds + slot + wa + (S) * 2048; lds_ptr xs_ = lds + slot + xa[S]; \
    b[F][0] = ldstr(ws_, ws_ + 512); a[F][0] = lds128(xs_); b[F][1] = ldstr(ws_ + 8192, ws_ + 8192 + 512); \
    a[F][1] = lds128(xs_ + 4096); a[F][2] = lds128(xs_ + 8192); a[F][3] = lds128(xs_ + 12288); \
    if (EXTRA) { lds_ptr es_ = lds + eslot + ea + (S) * 1024; be[F] = ldstr(es_, es_ + 256); ae[F] = lds128(xs_ + xe); } } while (0)
#define K1_MM(F) do { _Pragma("unroll") for (int mt = 0; mt < 4; ++mt) _Pragma("unroll") for (int nt = 0; nt < 2; ++nt) \
      acc[mt][nt] = SWAP ? MFMA32(b[F][nt], a[F][mt], acc[mt][nt]) : MFMA32(a[F][mt], b[F][nt], acc[mt][nt]); \
    if (EXTRA == 1) accx = MFMA32(be[F], ae[F], accx); if (EXTRA == 2) accx = MFMA32(ae[F], be[F], accx); } while (0)
#define SG(nd) do { __builtin_amdgcn_sched_group_barrier(0x100, nd, 0); __builtin_amdgcn_sched_group_barrier(0x8, 1, 0); } while (0)
#define K1_SCHED() do { SG(EXTRA ? 2 : 1); SG(EXTRA ? 2 : 1); SG(EXTRA ? 2 : 1); SG(1); SG(1); SG(1); SG(1); SG(1); \
    if (EXTRA) __builtin_amdgcn_sched_group_barrier(0x8, 1, 0); SBAR(); } while (0)
  BARRIER();
  K1_RD(0, 0);
  for (int st = 0; st < 16; ++st) {
    SBAR();
    K1_RD(1, 1); K1_MM(0); K1_SCHED();
    K1_RD(0, 2); K1_MM(1); K1_SCHED();
    K1_RD(1, 3); K1_MM(0); K1_SCHED();
    BARRIER();
    slot = (slot == 2 * K1_SLOT) ? 0 : slot + K1_SLOT; eslot ^= 8192;
    SBAR();
    K1_RD(0, 0); K1_MM(1); K1_SCHED();
  }
#undef K1_RD
#undef K1_MM
#undef K1_SCHED
}

__global__ __launch_bounds__(512, 2) void k1_proj(const char* __restrict__ xA, const char* __restrict__ wA, const char* __restrict__ xB, const char* __restrict__ wqB,
                                                  const char* __restrict__ wkB, const char* __restrict__ wvB, char* __restrict__ ws) {
  extern __shared__ __attribute__((aligned(16))) char smem[];
  lds_ptr lds = (lds_ptr)smem;
  const int lane = threadIdx.x & 63, wid = rfl(threadIdx.x >> 6);
  const int xcd = blockIdx.x & 7, idx = blockIdx.x >> 3, lr = idx & 3, lc = idx >> 2;
  const int rt = (xcd & 1) * 4 + lr, cg = xcd >> 1;
  const bool isB = cg == 3, isKV = cg < 2;
  STAMP(0, 0);
  if (wid >= 4) {
    const int lw = wid - 4;
    const unsigned lds0 = (unsigned)(uintptr_t)smem;
    const unsigned ldw2 = isB ? 2048 : 6144;
    const unsigned col0 = isKV ? (1024 + (cg * 8 + lc) * 64) * 2 : lc * 256, col1 = isKV ? col0 + 2048 : col0 + 128;
    const __amdgpu_buffer_rsrc_t sx = mk_srd(isB ? xB : xA, 4u << 20), sw = mk_srd(isB ? wqB : wA, isB ? (2u << 20) : (6u << 20));
    const __amdgpu_buffer_rsrc_t se = mk_srd(lw < 2 ? wkB : wvB, 65536);
    const unsigned vx0 = (lane >> 3) * 2048 + (((lane & 7) ^ (lane >> 4)) << 4), vx1 = (lane >> 3) * 2048 + (((lane & 7) ^ (4 + (lane >> 4))) << 4);
    const unsigned vw = (lane >> 3) * ldw2 + (((lane & 7) ^ (((lane >> 4) & 1) << 2)) << 4), ve = lane * 16;
    const unsigned sx0 = (rt * 256 + lw * 64) * 2048, sw0 = (lw & 1) * 32 * ldw2 + ((lw >> 1) ? col1 : col0);
    const unsigned dx = lds0 + lw * 8192, dw = lds0 + K1_XB + lw * 4096, de = lds0 + K1_KVOFF + lw * 2048;
    const int k0 = K0A;
    auto issue = [&](int st, unsigned slot) {
      const unsigned kt = (st + k0) & 15;
#pragma unroll
      for (int i = 0; i < 8; ++i) dma16(dx + slot + i * 1024, (i & 1) ? vx1 : vx0, sx, sx0 + kt * 128 + i * 16384);
#pragma unroll
      for (int i = 0; i < 4; ++i) dma16(dw + slot + i * 1024, vw, sw, sw0 + (kt * 64 + i * 8) * ldw2);
    };
    auto issue_e = [&](int st) {
      const unsigned kt = (st + k0) & 15;
#pragma unroll
      for (int i = 0; i < 2; ++i) dma16(de + (st & 1) * 8192 + i * 1024, ve, se, kt * 4096 + (lw & 1) * 2048 + i * 1024);
    };
    issue(0, 0); if (isB) issue_e(0);
    issue(1, K1_SLOT); if (isB) issue_e(1);
    issue(2, 2 * K1_SLOT);
    if (isB) WAITV(26); else WAITV(24);
    BARRIER();
    unsigned slot = 0;
    for (int st = 1; st < 15; ++st) {
      WAITV(12);
      BARRIER();
      if (isB) issue_e(st + 1);
      if (st < 14) issue(st + 2, slot); else WAITV(0);
      slot = (slot == 2 * K1_SLOT) ? 0 : slot + K1_SLOT;
    }
    BARRIER();
    BARRIER();
    STAMP(0, 3);
    return;
  }
  const int wr = wid >> 1, wc = wid & 1;
  f32x16 acc[4][2], accx;
#pragma unroll
  for (int mt = 0; mt < 4; ++mt) { acc[mt][0] = f32x16{}; acc[mt][1] = f32x16{}; }
  accx = f32x16{};
  if (isKV) {
    k1_mainloop<false, 0>(lds, wr, wc, lane, 0, acc, accx);
    STAMP(0, 2);
    f32x16 kv = {}, zz = {};
    const u32x4 onesw = {0x3f803f80u, 0x3f803f80u, 0x3f803f80u, 0x3f803f80u};
    const bf16x8 ones = __builtin_bit_cast(bf16x8, onesw);
#pragma unroll
    for (int mt = 0; mt < 4; ++mt) {
#pragma unroll
      for (int i = 0; i < 16; ++i) acc[mt][0][i] = phi(acc[mt][0][i]);
#pragma unroll
      for (int s = 0; s < 2; ++s) { const bf16x8 pk = pack8(acc[mt][0], 8 * s); kv = MFMA32(pk, pack8(acc[mt][1], 8 * s), kv); zz = MFMA32(pk, ones, zz); }
    }
    BARRIER();
    __attribute__((address_space(3))) f32x4* ex = (__attribute__((address_space(3))) f32x4*)(lds + wc * 8192) + lane;
    if (wr == 1) {
#pragma unroll
      for (int i = 0; i < 4; ++i) { ex[i * 64] = f32x4{kv[4 * i], kv[4 * i + 1], kv[4 * i + 2], kv[4 * i + 3]}; ex[256 + i * 64] = f32x4{zz[4 * i], zz[4 * i + 1], zz[4 * i + 2], zz[4 * i + 3]}; }
    }
    BARRIER();
    if (wr == 0) {
      const int head = (cg * 8 + lc) * 2 + wc;
      f32x2* kp = (f32x2*)(ws + WS_KVP + (size_t)(head * 8 + rt) * 4096) + lane;
      f32x4* zp = (f32x4*)(ws + WS_ZP + (size_t)(head * 8 + rt) * 128 + (lane >> 5) * 64);
#pragma unroll
      for (int i = 0; i < 4; ++i) {
        const f32x4 o = ex[i * 64], oz = ex[256 + i * 64];
        kp[(2 * i) * 64] = f32x2{kv[4 * i] + o[0], kv[4 * i + 1] + o[1]};
        kp[(2 * i + 1) * 64] = f32x2{kv[4 * i + 2] + o[2], kv[4 * i + 3] + o[3]};
        if ((lane & 31) == 0) zp[i] = f32x4{zz[4 * i] + oz[0], zz[4 * i + 1] + oz[1], zz[4 * i + 2] + oz[2], zz[4 * i + 3] + oz[3]};
      }
    }
    STAMP(0, 3);
    return;
  }
  const bool extra = isB && wr == (lc >> 2);
  if (!extra) k1_mainloop<true, 0>(lds, wr, wc, lane, 0, acc, accx);
  else if (wc == 0) k1_mainloop<true, 1>(lds, wr, wc, lane, lc * 32, acc, accx);
  else k1_mainloop<true, 2>(lds, wr, wc, lane, lc * 32, acc, accx);
  STAMP(0, 2);
  const float sc = isB ? QSCALE : 1.0f;
  char* qo = ws + (isB ? WS_QB : WS_QA) + ((size_t)(rt * 8 + wr * 4) * 32 + lc * 4 + wc) * 2048 + lane * 16;
#pragma unroll
  for (int mt = 0; mt < 4; ++mt)
#pragma unroll
    for (int nt = 0; nt < 2; ++nt) {
      const f32x16 v = acc[mt][nt] * sc;
      *(bf16x8*)(qo + mt * 65536 + nt * 4096) = pack8(v, 0);
      *(bf16x8*)(qo + mt * 65536 + nt * 4096 + 1024) = pack8(v, 8);
    }
  if (extra) {
    const int tt = rt * 8 + lc;
    char* ko = ws + WS_KVB + (wc == 0 ? ((tt + 62) & 63) * 4096 : tt * 4096 + 2048) + lane * 16;
    *(bf16x8*)ko = pack8(accx, 0);
    *(bf16x8*)(ko + 1024) = pack8(accx, 8);
  }
  STAMP(0, 3);
}

constexpr int K2_SLOT = 16384, K2_EX = 3 * K2_SLOT, K2_LDS = K2_EX + 4096;

DI void attn_exact(const char* kvb, bf16x8 q0, bf16x8 q1, int lane, char* dst) {
  f32x16 o = {}; float m = -INFINITY, l = 0.f;
  for (int t = 0; t < 64; ++t) {
    const char* kp = kvb + ((t + 62) & 63) * 4096 + lane * 16; const char* vp = kvb + t * 4096 + 2048 + lane * 16;
    f32x16 s = MFMA32(*(const bf16x8*)kp, q0, f32x16{}); s = MFMA32(*(const bf16x8*)(kp + 1024), q1, s);
    float mx = s[0];
#pragma unroll
    for (int i = 1; i < 16; ++i) mx = fmaxf(mx, s[i]);
    mx = fmaxf(mx, __shfl_xor(mx, 32));
    const float mn = fmaxf(m, mx), al = __builtin_amdgcn_exp2f(m - mn);
    float ps = 0.f;
#pragma unroll
    for (int i = 0; i < 16; ++i) { s[i] = __builtin_amdgcn_exp2f(s[i] - mn); ps += s[i]; }
    ps += __shfl_xor(ps, 32);
    l = l * al + ps; m = mn; o = o * al;
    o = MFMA32(*(const bf16x8*)vp, pack8(s, 0), o); o = MFMA32(*(const bf16x8*)(vp + 1024), pack8(s, 8), o);
  }
  o = o * (1.0f / l);
  *(bf16x8*)dst = pack8(o, 0); *(bf16x8*)(dst + 1024) = pack8(o, 8);
}

#define ATTN_ASM \
    "v_mfma_f32_32x32x16_bf16 v[128:143], %[k00], %[q0], %[negm]\n\t" \
    "v_mov_b32 v164, 0\n\t" \
    "v_mov_b32 v165, 0\n\t" \
    "v_mov_b32 v166, 0\n\t" \
    "v_mov_b32 v167, 0\n\t" \
    "v_mov_b32 v188, 0\n\t" \
    "v_mov_b32 v189, 0\n\t" \
    "v_mov_b32 v190, 0\n\t" \
    "v_mov_b32 v191, 0\n\t" \
    "v_mfma_f32_32x32x16_bf16 v[128:143], %[k01], %[q1], v[128:143]\n\t" \
    "ds_read_b128 v[168:171], %[ka]\n\t" \
    "ds_read_b128 v[172:175], %[ka] offset:1024\n\t" \
    "v_mov_b32 v196, %[la]\n\t" \
    "s_mov_b32 %[cnt], 0\n\t" \
    "s_mov_b32 %[slot], 0\n\t" \
    "s_nop 7\n\t" \
    "s_nop 7\n\t" \
    "1:\n\t" \
    "ds_read_b128 v[184:187], v196 offset:2048\n\t" \
    "ds_read_b128 v[192:195], v196 offset:3072\n\t" \
    "ds_read_b128 v[176:179], v196 offset:0\n\t" \
    "ds_read_b128 v[180:183], v196 offset:1024\n\t" \
    "s_waitcnt lgkmcnt(4)\n\t" \
    "v_mfma_f32_32x32x16_bf16 %[o], v[188:191], v[164:167], %[o]\n\t" \
    "v_exp_f32_e32 v128, v128\n\t" \
    "v_exp_f32_e32 v129, v129\n\t" \
    "v_exp_f32_e32 v130, v130\n\t" \
    "v_mfma_f32_32x32x16_bf16 v[144:159], v[168:171], %[q0], %[negm]\n\t" \
    "v_exp_f32_e32 v131, v131\n\t" \
    "v_exp_f32_e32 v132, v132\n\t" \
    "v_exp_f32_e32 v133, v133\n\t" \
    "v_mfma_f32_16x16x32_bf16 %[l], %[ones], v[164:167], %[l]\n\t" \
    "v_cvt_pk_bf16_f32 v160, v128, v129\n\t" \
    "v_cvt_pk_bf16_f32 v161, v130, v131\n\t" \
    "v_exp_f32_e32 v134, v134\n\t" \
    "v_mfma_f32_32x32x16_bf16 v[144:159], v[172:175], %[q1], v[144:159]\n\t" \
    "v_exp_f32_e32 v135, v135\n\t" \
    "v_cvt_pk_bf16_f32 v162, v132, v133\n\t" \
    "v_exp_f32_e32 v136, v136\n\t" \
    "v_cvt_pk_bf16_f32 v163, v134, v135\n\t" \
    "v_exp_f32_e32 v137, v137\n\t" \
    "s_waitcnt lgkmcnt(3)\n\t" \
    "v_mfma_f32_32x32x16_bf16 %[o], v[184:187], v[160:163], %[o]\n\t" \
    "v_exp_f32_e32 v138, v138\n\t" \
    "v_cvt_pk_bf16_f32 v164, v136, v137\n\t" \
    "v_exp_f32_e32 v139, v139\n\t" \
    "v_exp_f32_e32 v140, v140\n\t" \
    "v_mfma_f32_16x16x32_bf16 %[l], %[ones], v[160:163], %[l]\n\t" \
    "v_cvt_pk_bf16_f32 v165, v138, v139\n\t" \
    "v_exp_f32_e32 v141, v141\n\t" \
    "v_exp_f32_e32 v142, v142\n\t" \
    "v_exp_f32_e32 v143, v143\n\t" \
    "v_cvt_pk_bf16_f32 v166, v140, v141\n\t" \
    "v_cvt_pk_bf16_f32 v167, v142, v143\n\t" \
    "ds_read_b128 v[184:187], v196 offset:6144\n\t" \
    "ds_read_b128 v[188:191], v196 offset:7168\n\t" \
    "ds_read_b128 v[168:171], v196 offset:4096\n\t" \
    "ds_read_b128 v[172:175], v196 offset:5120\n\t" \
    "s_waitcnt lgkmcnt(4)\n\t" \
    "v_mfma_f32_32x32x16_bf16 %[o], v[192:195], v[164:167], %[o]\n\t" \
    "v_exp_f32_e32 v144, v144\n\t" \
    "v_exp_f32_e32 v145, v145\n\t" \
    "v_exp_f32_e32 v146, v146\n\t" \
    "v_mfma_f32_32x32x16_bf16 v[128:143], v[176:179], %[q0], %[negm]\n\t" \
    "v_exp_f32_e32 v147, v147\n\t" \
    "v_exp_f32_e32 v148, v148\n\t" \
    "v_exp_f32_e32 v149, v149\n\t" \
    "v_mfma_f32_16x16x32_bf16 %[l], %[ones], v[164:167], %[l]\n\t" \
    "v_cvt_pk_bf16_f32 v160, v144, v145\n\t" \
    "v_cvt_pk_bf16_f32 v161, v146, v147\n\t" \
    "v_exp_f32_e32 v150, v150\n\t" \
    "v_mfma_f32_32x32x16_bf16 v[128:143], v[180:183], %[q1], v[128:143]\n\t" \
    "v_exp_f32_e32 v151, v151\n\t" \
    "v_cvt_pk_bf16_f32 v162, v148, v149\n\t" \
    "v_exp_f32_e32 v152, v152\n\t" \
    "v_cvt_pk_bf16_f32 v163, v150, v151\n\t" \
    "v_exp_f32_e32 v153, v153\n\t" \
    "s_waitcnt lgkmcnt(3)\n\t" \
    "v_mfma_f32_32x32x16_bf16 %[o], v[184:187], v[160:163], %[o]\n\t" \
    "v_exp_f32_e32 v154, v154\n\t" \
    "v_cvt_pk_bf16_f32 v164, v152, v153\n\t" \
    "v_exp_f32_e32 v155, v155\n\t" \
    "v_exp_f32_e32 v156, v156\n\t" \
    "v_mfma_f32_16x16x32_bf16 %[l], %[ones], v[160:163], %[l]\n\t" \
    "v_cvt_pk_bf16_f32 v165, v154, v155\n\t" \
    "v_exp_f32_e32 v157, v157\n\t" \
    "v_exp_f32_e32 v158, v158\n\t" \
    "v_exp_f32_e32 v159, v159\n\t" \
    "v_cvt_pk_bf16_f32 v166, v156, v157\n\t" \
    "v_cvt_pk_bf16_f32 v167, v158, v159\n\t" \
    "ds_read_b128 v[184:187], v196 offset:10240\n\t" \
    "ds_read_b128 v[192:195], v196 offset:11264\n\t" \
    "ds_read_b128 v[176:179], v196 offset:8192\n\t" \
    "ds_read_b128 v[180:183], v196 offset:9216\n\t" \
    "s_waitcnt lgkmcnt(4)\n\t" \
    "v_mfma_f32_32x32x16_bf16 %[o], v[188:191], v[164:167], %[o]\n\t" \
    "v_exp_f32_e32 v128, v128\n\t" \
    "v_exp_f32_e32 v129, v129\n\t" \
    "v_exp_f32_e32 v130, v130\n\t" \
    "v_mfma_f32_32x32x16_bf16 v[144:159], v[168:171], %[q0], %[negm]\n\t" \
    "v_exp_f32_e32 v131, v131\n\t" \
    "v_exp_f32_e32 v132, v132\n\t" \
    "v_exp_f32_e32 v133, v133\n\t" \
    "v_mfma_f32_16x16x32_bf16 %[l], %[ones], v[164:167], %[l]\n\t" \
    "v_cvt_pk_bf16_f32 v160, v128, v129\n\t" \
    "v_cvt_pk_bf16_f32 v161, v130, v131\n\t" \
    "v_exp_f32_e32 v134, v134\n\t" \
    "v_mfma_f32_32x32x16_bf16 v[144:159], v[172:175], %[q1], v[144:159]\n\t" \
    "v_exp_f32_e32 v135, v135\n\t" \
    "v_cvt_pk_bf16_f32 v162, v132, v133\n\t" \
    "v_exp_f32_e32 v136, v136\n\t" \
    "v_cvt_pk_bf16_f32 v163, v134, v135\n\t" \
    "v_exp_f32_e32 v137, v137\n\t" \
    "s_waitcnt lgkmcnt(3)\n\t" \
    "v_mfma_f32_32x32x16_bf16 %[o], v[184:187], v[160:163], %[o]\n\t" \
    "v_exp_f32_e32 v138, v138\n\t" \
    "v_cvt_pk_bf16_f32 v164, v136, v137\n\t" \
    "v_exp_f32_e32 v139, v139\n\t" \
    "v_exp_f32_e32 v140, v140\n\t" \
    "v_mfma_f32_16x16x32_bf16 %[l], %[ones], v[160:163], %[l]\n\t" \
    "v_cvt_pk_bf16_f32 v165, v138, v139\n\t" \
    "v_exp_f32_e32 v141, v141\n\t" \
    "v_exp_f32_e32 v142, v142\n\t" \
    "v_exp_f32_e32 v143, v143\n\t" \
    "v_cvt_pk_bf16_f32 v166, v140, v141\n\t" \
    "v_cvt_pk_bf16_f32 v167, v142, v143\n\t" \
    "ds_read_b128 v[184:187], v196 offset:14336\n\t" \
    "ds_read_b128 v[188:191], v196 offset:15360\n\t" \
    "ds_read_b128 v[168:171], v196 offset:12288\n\t" \
    "ds_read_b128 v[172:175], v196 offset:13312\n\t" \
    "s_waitcnt lgkmcnt(4)\n\t" \
    "v_mfma_f32_32x32x16_bf16 %[o], v[192:195], v[164:167], %[o]\n\t" \
    "v_exp_f32_e32 v144, v144\n\t" \
    "v_exp_f32_e32 v145, v145\n\t" \
    "v_exp_f32_e32 v146, v146\n\t" \
    "v_mfma_f32_32x32x16_bf16 v[128:143], v[176:179], %[q0], %[negm]\n\t" \
    "v_exp_f32_e32 v147, v147\n\t" \
    "v_exp_f32_e32 v148, v148\n\t" \
    "v_exp_f32_e32 v149, v149\n\t" \
    "v_mfma_f32_16x16x32_bf16 %[l], %[ones], v[164:167], %[l]\n\t" \
    "v_cvt_pk_bf16_f32 v160, v144, v145\n\t" \
    "v_cvt_pk_bf16_f32 v161, v146, v147\n\t" \
    "v_exp_f32_e32 v150, v150\n\t" \
    "v_mfma_f32_32x32x16_bf16 v[128:143], v[180:183], %[q1], v[128:143]\n\t" \
    "v_exp_f32_e32 v151, v151\n\t" \
    "v_cvt_pk_bf16_f32 v162, v148, v149\n\t" \
    "v_exp_f32_e32 v152, v152\n\t" \
    "v_cvt_pk_bf16_f32 v163, v150, v151\n\t" \
    "v_exp_f32_e32 v153, v153\n\t" \
    "s_waitcnt lgkmcnt(3)\n\t" \
    "v_mfma_f32_32x32x16_bf16 %[o], v[184:187], v[160:163], %[o]\n\t" \
    "v_exp_f32_e32 v154, v154\n\t" \
    "v_cvt_pk_bf16_f32 v164, v152, v153\n\t" \
    "v_exp_f32_e32 v155, v155\n\t" \
    "v_exp_f32_e32 v156, v156\n\t" \
    "v_mfma_f32_16x16x32_bf16 %[l], %[ones], v[160:163], %[l]\n\t" \
    "v_cvt_pk_bf16_f32 v165, v154, v155\n\t" \
    "v_exp_f32_e32 v157, v157\n\t" \
    "v_exp_f32_e32 v158, v158\n\t" \
    "v_exp_f32_e32 v159, v159\n\t" \
    "v_cvt_pk_bf16_f32 v166, v156, v157\n\t" \
    "v_cvt_pk_bf16_f32 v167, v158, v159\n\t" \
    "s_cmp_lt_u32 %[cnt], 13\n\t" \
    "s_cbranch_scc1 2f\n\t" \
    "s_waitcnt vmcnt(0) lgkmcnt(0)\n\t" \
    "s_barrier\n\t" \
    "s_branch 3f\n\t" \
    "2:\n\t" \
    "s_waitcnt vmcnt(2) lgkmcnt(0)\n\t" \
    "s_barrier\n\t" \
    "s_add_u32 m0, %[dkv], %[slot]\n\t" \
    "s_nop 0\n\t" \
    "buffer_load_dwordx4 %[vkv], %[srd], %[soff] offen lds\n\t" \
    "buffer_load_dwordx4 %[vkv], %[srd], %[soff] offen offset:1024 lds\n\t" \
    "s_add_u32 %[soff], %[soff], 0x4000\n\t" \
    "3:\n\t" \
    "s_add_u32 %[slot], %[slot], 0x4000\n\t" \
    "s_cmp_eq_u32 %[slot], 0xc000\n\t" \
    "s_cselect_b32 %[slot], 0, %[slot]\n\t" \
    "v_add_u32 v196, %[slot], %[la]\n\t" \
    "s_add_u32 %[cnt], %[cnt], 1\n\t" \
    "s_cmp_lt_u32 %[cnt], 16\n\t" \
    "s_cbranch_scc1 1b\n\t" \
    "v_mfma_f32_32x32x16_bf16 %[o], v[188:191], v[164:167], %[o]\n\t" \
    "v_mfma_f32_16x16x32_bf16 %[l], %[ones], v[164:167], %[l]\n\t" \
    "s_nop 7\n\t" \
    "s_nop 7\n\t"
#define ATTN_CLOB "v128", "v129", "v130", "v131", "v132", "v133", "v134", "v135", "v136", "v137", "v138", "v139", "v140", "v141", "v142", "v143", "v144", "v145", "v146", "v147", "v148", "v149", "v150", "v151", "v152", "v153", "v154", "v155", "v156", "v157", "v158", "v159", "v160", "v161", "v162", "v163", "v164", "v165", "v166", "v167", "v168", "v169", "v170", "v171", "v172", "v173", "v174", "v175", "v176", "v177", "v178", "v179", "v180", "v181", "v182", "v183", "v184", "v185", "v186", "v187", "v188", "v189", "v190", "v191", "v192", "v193", "v194", "v195", "v196"

__global__ __launch_bounds__(512, 2) void k2_attn(char* __restrict__ ws) {
  extern __shared__ __attribute__((aligned(16))) char smem[];
  lds_ptr lds = (lds_ptr)smem;
  const int lane = threadIdx.x & 63, wid = rfl(threadIdx.x >> 6), h = lane >> 5;
  const int head = blockIdx.x & 31, tt = (blockIdx.x >> 5) * 8 + wid;
  const unsigned lds0 = (unsigned)(uintptr_t)smem;
  const char* kvb = ws + WS_KVB;
  const __amdgpu_buffer_rsrc_t skv = mk_srd(kvb, 262144);
  const unsigned vkv = lane * 16, dkv = lds0 + wid * 2048;
#define K2_ISSUE(st, slot) do { dma16(dkv + (slot), vkv, skv, (st) * 16384 + wid * 2048); dma16(dkv + (slot) + 1024, vkv, skv, (st) * 16384 + wid * 2048 + 1024); } while (0)
  const size_t fo = ((size_t)tt * 32 + head) * 2048 + lane * 16;
  STAMP(1, 0);
  if (wid < 4) dma16(lds0 + K2_EX + wid * 1024, vkv, skv, (62 + (wid >> 1)) * 4096 + (wid & 1) * 1024);
  K2_ISSUE(0, 0);
  const bf16x8 q0 = *(const bf16x8*)(ws + WS_QB + fo), q1 = *(const bf16x8*)(ws + WS_QB + fo + 1024);
  WAITV(0);
  BARRIER();
  K2_ISSUE(1, K2_SLOT);
  const u32x4 qa0 = *(const u32x4*)(ws + WS_QA + fo), qa1 = *(const u32x4*)(ws + WS_QA + fo + 1024);
  f32x2 pa[8], pb[8];
  {
    const f32x2* kp = (const f32x2*)(ws + WS_KVP + (size_t)head * 32768 + wid * 512) + lane;
    const f32x2* zp = (const f32x2*)(ws + WS_ZP + (size_t)head * 1024 + h * 64 + wid * 8);
#pragma unroll
    for (int i = 0; i < 8; ++i) { pa[i] = kp[i * 512]; pb[i] = zp[i * 16]; }
  }
  K2_ISSUE(2, 2 * K2_SLOT);
  const bf16x8 k00 = lds128(lds + K2_EX + lane * 16), k01 = lds128(lds + K2_EX + 1024 + lane * 16);
  f32x16 negm = MFMA32(k00, q0, f32x16{}); negm = MFMA32(k01, q1, negm);
  float m0 = fmaxf(fmaxf(negm[0], negm[1]), negm[2]);
#pragma unroll
  for (int i = 3; i < 15; i += 2) m0 = fmaxf(fmaxf(m0, negm[i]), negm[i + 1]);
  m0 = fmaxf(m0, negm[15]);
  { auto rr = __builtin_amdgcn_permlane32_swap(__float_as_uint(m0), __float_as_uint(m0), false, false); m0 = fmaxf(__uint_as_float(rr[0]), __uint_as_float(rr[1])); }
#pragma unroll
  for (int i = 0; i < 16; ++i) negm[i] = -m0;
  const unsigned onev = (((lane >> 4) ^ lane) & 1) ? 0u : 0x3f803f80u;
  const u32x4 ones = {onev, onev, onev, onev};
  f32x16 o = {}; f32x4 l = {};
  STAMP(1, 1);
  {
    unsigned cnt, slot, soff = 3 * 16384 + wid * 2048;
    asm volatile("s_nop 1\n\t" ATTN_ASM
                 : [o] "+v"(o), [l] "+v"(l), [cnt] "=&s"(cnt), [slot] "=&s"(slot), [soff] "+s"(soff)
                 : [q0] "v"(q0), [q1] "v"(q1), [negm] "v"(negm), [ones] "v"(ones), [k00] "v"(k00), [k01] "v"(k01), [ka] "v"(lds0 + K2_EX + 2048 + lane * 16),
                   [la] "v"(lds0 + lane * 16), [vkv] "v"(vkv), [srd] "s"(skv), [dkv] "s"(dkv)
                 : "memory", "scc", ATTN_CLOB);
  }
  STAMP(1, 2);
  const float ls = (lane & 16) ? l[1] : l[0];
  o = o * __builtin_amdgcn_rcpf(ls);
  float chk = 0.f;
#pragma unroll
  for (int i = 0; i < 16; ++i) chk = __builtin_fmaf(o[i], 0.f, chk);
  const bool bad = !(ls < 1.2676506e30f) || !(chk == 0.f);
  char* dst = ws + WS_OB + fo;
  if (__builtin_expect(__any(bad), 0)) attn_exact(kvb, q0, q1, lane, dst);
  else { *(bf16x8*)dst = pack8(o, 0); *(bf16x8*)(dst + 1024) = pack8(o, 8); }
  f32x2 kvs = pa[0], zs = pb[0];
#pragma unroll
  for (int i = 1; i < 8; ++i) { kvs += pa[i]; zs += pb[i]; }
  *(__attribute__((address_space(3))) unsigned*)(lds + K2_EX + (wid >> 2) * 1024 + lane * 16 + (wid & 3) * 4) = cvtpk(kvs[0], kvs[1]);
  *(__attribute__((address_space(3))) unsigned*)(lds + K2_EX + 2048 + (wid >> 2) * 1024 + lane * 16 + (wid & 3) * 4) = cvtpk(zs[0], zs[1]);
  f32x16 pq;
#pragma unroll
  for (int i = 0; i < 4; ++i) {
    pq[2 * i] = phi(__uint_as_float(qa0[i] << 16)); pq[2 * i + 1] = phi(__uint_as_float(qa0[i] & 0xffff0000u));
    pq[8 + 2 * i] = phi(__uint_as_float(qa1[i] << 16)); pq[8 + 2 * i + 1] = phi(__uint_as_float(qa1[i] & 0xffff0000u));
  }
  const bf16x8 pq0 = pack8(pq, 0), pq1 = pack8(pq, 8);
  BARRIER();
  {
    lds_ptr e = lds + K2_EX + lane * 16;
    f32x16 num = MFMA32(lds128(e), pq0, f32x16{}); num = MFMA32(lds128(e + 1024), pq1, num);
    f32x16 den = MFMA32(lds128(e + 2048), pq0, f32x16{}); den = MFMA32(lds128(e + 3072), pq1, den);
    num = num * __builtin_amdgcn_rcpf(den[0] + 1e-6f);
    *(bf16x8*)(ws + WS_OA + fo) = pack8(num, 0); *(bf16x8*)(ws + WS_OA + fo + 1024) = pack8(num, 8);
  }
  STAMP(1, 3);
#undef K2_ISSUE
}

constexpr int K3_SLOT = 32768, K3_NS = 4, K3_LDS = K3_NS * K3_SLOT;

__global__ __launch_bounds__(512, 2) void k3_out(const char* __restrict__ ws, const char* __restrict__ woA, const char* __restrict__ woB, char* __restrict__ out) {
  extern __shared__ __attribute__((aligned(16))) char smem[];
  lds_ptr lds = (lds_ptr)smem;
  const int lane = threadIdx.x & 63, wid = rfl(threadIdx.x >> 6);
  const int xcd = blockIdx.x & 7, idx = blockIdx.x >> 3, lr = idx & 3, lc = idx >> 2;
  const int br = xcd >> 2, rt = (xcd & 3) * 4 + lr;
  STAMP(2, 0);
  if (wid >= 4) {
    const int lw = wid - 4;
    const unsigned lds0 = (unsigned)(uintptr_t)smem;
    const __amdgpu_buffer_rsrc_t sa = mk_srd(ws + (br ? WS_OB : WS_OA), 4u << 20), sw = mk_srd(br ? woB : woA, 2u << 20);
    const unsigned va = lane * 16, vw = (lane >> 3) * 2048 + (((lane & 7) ^ (((lane >> 4) & 1) << 2)) << 4);
    const unsigned sa0 = (rt * 4 + lw) * 65536, sw0 = (lw & 1) * 32 * 2048 + lc * 256 + (lw >> 1) * 128;
    const unsigned da = lds0 + lw * 4096, dw = lds0 + 16384 + lw * 4096;
    const int k0 = K0B;
    auto issue = [&](int st) {
      const unsigned kt = (st + k0) & 15, slot = (st & 3) * K3_SLOT;
#pragma unroll
      for (int i = 0; i < 4; ++i) dma16(da + slot + i * 1024, va, sa, sa0 + kt * 4096 + i * 1024);
#pragma unroll
      for (int i = 0; i < 4; ++i) dma16(dw + slot + i * 1024, vw, sw, sw0 + (kt * 64 + i * 8) * 2048);
    };
    issue(0); issue(1); issue(2); issue(3);
    WAITV(24);
    BARRIER();
    for (int st = 1; st < 13; ++st) { WAITV(16); BARRIER(); issue(st + 3); }
    WAITV(16); BARRIER();
    WAITV(8); BARRIER();
    WAITV(0); BARRIER();
    BARRIER();
    return;
  }
  const int wr = wid >> 1, wc = wid & 1;
  const int h = lane >> 5, q = (lane & 15) >> 2, p = lane & 3, blk = (lane >> 4) & 1;
  const unsigned aa = wr * 8192 + lane * 16;
  const unsigned w0 = 16384 + wc * 8192 + (4 * h + q) * 128 + ((q >> 1) << 6) + blk * 32 + p * 8, w1 = w0 ^ 64;
  f32x16 acc[2][2] = {};
  bf16x8 a[2][2], b[2][2];
  unsigned slot = 0;
#define K3_RD(F, S) do { lds_ptr a_ = lds + slot + aa + (S) * 1024; lds_ptr b0_ = lds + slot + w0 + (S) * 2048; lds_ptr b1_ = lds + slot + w1 + (S) * 2048; \
    b[F][0] = ldstr(b0_, b0_ + 1024); a[F][0] = lds128(a_); b[F][1] = ldstr(b1_, b1_ + 1024); a[F][1] = lds128(a_ + 4096); } while (0)
#define K3_MM(F) do { _Pragma("unroll") for (int mt = 0; mt < 2; ++mt) _Pragma("unroll") for (int nt = 0; nt < 2; ++nt) acc[mt][nt] = MFMA32(b[F][nt], a[F][mt], acc[mt][nt]); } while (0)
#define K3_SCHED() do { SG(2); SG(2); SG(1); SG(1); SBAR(); } while (0)
  BARRIER();
  K3_RD(0, 0);
  for (int st = 0; st < 16; ++st) {
    SBAR();
    K3_RD(1, 1); K3_MM(0); K3_SCHED();
    K3_RD(0, 2); K3_MM(1); K3_SCHED();
    K3_RD(1, 3); K3_MM(0); K3_SCHED();
    BARRIER();
    slot = (slot + K3_SLOT) & (K3_LDS - 1);
    SBAR();
    K3_RD(0, 0); K3_MM(1); K3_SCHED();
  }
  STAMP(2, 2);
  char* o = out + (size_t)(rt * 128 + wr * 64 + (lane & 31)) * 4096 + br * 2048 + lc * 256 + wc * 128 + h * 16;
#pragma unroll
  for (int mt = 0; mt < 2; ++mt)
#pragma unroll
    for (int nt = 0; nt < 2; ++nt)
#pragma unroll
      for (int g = 0; g < 2; ++g) {
        const f32x16& v = acc[mt][nt];
        unsigned x0 = cvtpk(v[8 * g], v[8 * g + 1]), x1 = cvtpk(v[8 * g + 2], v[8 * g + 3]), y0 = cvtpk(v[8 * g + 4], v[8 * g + 5]), y1 = cvtpk(v[8 * g + 6], v[8 * g + 7]);
        auto r0 = __builtin_amdgcn_permlane32_swap(x0, y0, false, false); auto r1 = __builtin_amdgcn_permlane32_swap(x1, y1, false, false);
        *(u32x4*)(o + mt * 32 * 4096 + nt * 64 + g * 32) = u32x4{r0[0], r1[0], r0[1], r1[1]};
      }
  STAMP(2, 3);
}

extern "C" void kernel_launch(void* const* d_in, const int* in_sizes, int n_in, void* d_out, int out_size, void* d_ws, size_t ws_size, hipStream_t stream) {
  (void)hipFuncSetAttribute((const void*)k1_proj, hipFuncAttributeMaxDynamicSharedMemorySize, K1_LDS);
  (void)hipFuncSetAttribute((const void*)k2_attn, hipFuncAttributeMaxDynamicSharedMemorySize, K2_LDS);
  (void)hipFuncSetAttribute((const void*)k3_out, hipFuncAttributeMaxDynamicSharedMemorySize, K3_LDS);
  char* ws = (char*)d_ws;
  k1_proj<<<256, 512, K1_LDS, stream>>>((const char*)d_in[0], (const char*)d_in[1], (const char*)d_in[3], (const char*)d_in[4], (const char*)d_in[5], (const char*)d_in[6], ws);
  k2_attn<<<256, 512, K2_LDS, stream>>>(ws);
  k3_out<<<256, 512, K3_LDS, stream>>>(ws, (const char*)d_in[2], (const char*)d_in[7], (char*)d_out);
}
```

```cpp
#include <hip/hip_runtime.h>
#include <hip/hip_bf16.h>
#include <stdint.h>
#define GEMM_H "gemm32.h"
#define K0A 0
#define K0B 0
#define ATTN_H "attn_asm.h"

typedef __bf16 bf16x8 __attribute__((ext_vector_type(8)));
typedef __bf16 bf16x2 __attribute__((ext_vector_type(2)));
typedef short s16x4 __attribute__((ext_vector_type(4)));
typedef float f32x16 __attribute__((ext_vector_type(16)));
typedef float f32x4 __attribute__((ext_vector_type(4)));
typedef float f32x2 __attribute__((ext_vector_type(2)));
typedef unsigned u32x4 __attribute__((ext_vector_type(4)));
typedef unsigned u32x2 __attribute__((ext_vector_type(2)));
typedef __attribute__((address_space(3))) char* lds_ptr;
#define DI __device__ __forceinline__
#define SBAR() __builtin_amdgcn_sched_barrier(0)
#define PIN(x) asm volatile("" : "+v"(x))
#define MFMA32(a, b, c) __builtin_amdgcn_mfma_f32_32x32x16_bf16(a, b, c, 0, 0, 0)
#define MFMA16(a, b, c) __builtin_amdgcn_mfma_f32_16x16x32_bf16(a, b, c, 0, 0, 0)

constexpr size_t WS_QB = 0, WS_QA = 4u << 20, WS_OA = 8u << 20, WS_OB = 12u << 20, WS_KVB = 16u << 20, WS_KVP = 17u << 20, WS_ZP = 18u << 20;
constexpr float QSCALE = 0.17677669529663687f * 1.4426950408889634f;
constexpr float LOG2E = 1.4426950408889634f;

#define STAMP(k, i)
DI unsigned rfl(unsigned x) { return __builtin_amdgcn_readfirstlane(x); }
DI unsigned cvtpk(float lo, float hi) { f32x2 v = {lo, hi}; return __builtin_bit_cast(unsigned, __builtin_convertvector(v, bf16x2)); }
DI bf16x8 pack8(const f32x16& v, int o) { u32x4 w = {cvtpk(v[o], v[o + 1]), cvtpk(v[o + 2], v[o + 3]), cvtpk(v[o + 4], v[o + 5]), cvtpk(v[o + 6], v[o + 7])}; return __builtin_bit_cast(bf16x8, w); }
DI __amdgpu_buffer_rsrc_t mk_srd(const void* p, unsigned bytes) { return __builtin_amdgcn_make_buffer_rsrc((void*)p, 0, bytes, 0x00020000); }
DI void dma16(unsigned ldsaddr, unsigned voff, __amdgpu_buffer_rsrc_t srd, unsigned soff) {
  asm volatile("s_mov_b32 m0, %0\n\ts_nop 0\n\tbuffer_load_dwordx4 %1, %2, %3 offen lds" ::"s"(ldsaddr), "v"(voff), "s"(srd), "s"(soff) : "memory");
}
#define WAITV(n) asm volatile("s_waitcnt vmcnt(" #n ")" ::: "memory")
#define BARRIER() asm volatile("s_waitcnt lgkmcnt(0)\n\ts_barrier" ::: "memory")
DI bf16x8 lds128(lds_ptr p) { return *(const __attribute__((address_space(3))) bf16x8*)p; }
DI bf16x8 ldstr(lds_ptr p0, lds_ptr p1) {
  typedef short v4i16 __attribute__((ext_vector_type(4)));
  v4i16 a = __builtin_amdgcn_ds_read_tr16_b64_v4i16((__attribute__((address_space(3))) v4i16*)p0);
  v4i16 b = __builtin_amdgcn_ds_read_tr16_b64_v4i16((__attribute__((address_space(3))) v4i16*)p1);
  return __builtin_bit_cast(bf16x8, __builtin_shufflevector(a, b, 0, 1, 2, 3, 4, 5, 6, 7));
}
DI float phi(float t) { float e = __builtin_amdgcn_fmed3f(__builtin_amdgcn_exp2f(t * LOG2E), 0.f, 1.f); return __builtin_fmaxf(t, 0.f) + (e + 1e-6f); }

constexpr int K1_SLOT = 49152, K1_XB = 32768, K1_KVOFF = 3 * K1_SLOT, K1_LDS = K1_KVOFF + 2 * 8192;

#define G1KV_ASM \
    "v_mov_b32 v0, 0\n\t" \
    "v_mov_b32 v1, 0\n\t" \
    "v_mov_b32 v2, 0\n\t" \
    "v_mov_b32 v3, 0\n\t" \
    "v_mov_b32 v4, 0\n\t" \
    "v_mov_b32 v5, 0\n\t" \
    "v_mov_b32 v6, 0\n\t" \
    "v_mov_b32 v7, 0\n\t" \
    "v_mov_b32 v8, 0\n\t" \
    "v_mov_b32 v9, 0\n\t" \
    "v_mov_b32 v10, 0\n\t" \
    "v_mov_b32 v11, 0\n\t" \
    "v_mov_b32 v12, 0\n\t" \
    "v_mov_b32 v13, 0\n\t" \
    "v_mov_b32 v14, 0\n\t" \
    "v_mov_b32 v15, 0\n\t" \
    "v_mov_b32 v16, 0\n\t" \
    "v_mov_b32 v17, 0\n\t" \
    "v_mov_b32 v18, 0\n\t" \
    "v_mov_b32 v19, 0\n\t" \
    "v_mov_b32 v20, 0\n\t" \
    "v_mov_b32 v21, 0\n\t" \
    "v_mov_b32 v22, 0\n\t" \
    "v_mov_b32 v23, 0\n\t" \
    "v_mov_b32 v24, 0\n\t" \
    "v_mov_b32 v25, 0\n\t" \
    "v_mov_b32 v26, 0\n\t" \
    "v_mov_b32 v27, 0\n\t" \
    "v_mov_b32 v28, 0\n\t" \
    "v_mov_b32 v29, 0\n\t" \
    "v_mov_b32 v30, 0\n\t" \
    "v_mov_b32 v31, 0\n\t" \
    "v_mov_b32 v32, 0\n\t" \
    "v_mov_b32 v33, 0\n\t" \
    "v_mov_b32 v34, 0\n\t" \
    "v_mov_b32 v35, 0\n\t" \
    "v_mov_b32 v36, 0\n\t" \
    "v_mov_b32 v37, 0\n\t" \
    "v_mov_b32 v38, 0\n\t" \
    "v_mov_b32 v39, 0\n\t" \
    "v_mov_b32 v40, 0\n\t" \
    "v_mov_b32 v41, 0\n\t" \
    "v_mov_b32 v42, 0\n\t" \
    "v_mov_b32 v43, 0\n\t" \
    "v_mov_b32 v44, 0\n\t" \
    "v_mov_b32 v45, 0\n\t" \
    "v_mov_b32 v46, 0\n\t" \
    "v_mov_b32 v47, 0\n\t" \
    "v_mov_b32 v48, 0\n\t" \
    "v_mov_b32 v49, 0\n\t" \
    "v_mov_b32 v50, 0\n\t" \
    "v_mov_b32 v51, 0\n\t" \
    "v_mov_b32 v52, 0\n\t" \
    "v_mov_b32 v53, 0\n\t" \
    "v_mov_b32 v54, 0\n\t" \
    "v_mov_b32 v55, 0\n\t" \
    "v_mov_b32 v56, 0\n\t" \
    "v_mov_b32 v57, 0\n\t" \
    "v_mov_b32 v58, 0\n\t" \
    "v_mov_b32 v59, 0\n\t" \
    "v_mov_b32 v60, 0\n\t" \
    "v_mov_b32 v61, 0\n\t" \
    "v_mov_b32 v62, 0\n\t" \
    "v_mov_b32 v63, 0\n\t" \
    "v_mov_b32 v64, 0\n\t" \
    "v_mov_b32 v65, 0\n\t" \
    "v_mov_b32 v66, 0\n\t" \
    "v_mov_b32 v67, 0\n\t" \
    "v_mov_b32 v68, 0\n\t" \
    "v_mov_b32 v69, 0\n\t" \
    "v_mov_b32 v70, 0\n\t" \
    "v_mov_b32 v71, 0\n\t" \
    "v_mov_b32 v72, 0\n\t" \
    "v_mov_b32 v73, 0\n\t" \
    "v_mov_b32 v74, 0\n\t" \
    "v_mov_b32 v75, 0\n\t" \
    "v_mov_b32 v76, 0\n\t" \
    "v_mov_b32 v77, 0\n\t" \
    "v_mov_b32 v78, 0\n\t" \
    "v_mov_b32 v79, 0\n\t" \
    "v_mov_b32 v80, 0\n\t" \
    "v_mov_b32 v81, 0\n\t" \
    "v_mov_b32 v82, 0\n\t" \
    "v_mov_b32 v83, 0\n\t" \
    "v_mov_b32 v84, 0\n\t" \
    "v_mov_b32 v85, 0\n\t" \
    "v_mov_b32 v86, 0\n\t" \
    "v_mov_b32 v87, 0\n\t" \
    "v_mov_b32 v88, 0\n\t" \
    "v_mov_b32 v89, 0\n\t" \
    "v_mov_b32 v90, 0\n\t" \
    "v_mov_b32 v91, 0\n\t" \
    "v_mov_b32 v92, 0\n\t" \
    "v_mov_b32 v93, 0\n\t" \
    "v_mov_b32 v94, 0\n\t" \
    "v_mov_b32 v95, 0\n\t" \
    "v_mov_b32 v96, 0\n\t" \
    "v_mov_b32 v97, 0\n\t" \
    "v_mov_b32 v98, 0\n\t" \
    "v_mov_b32 v99, 0\n\t" \
    "v_mov_b32 v100, 0\n\t" \
    "v_mov_b32 v101, 0\n\t" \
    "v_mov_b32 v102, 0\n\t" \
    "v_mov_b32 v103, 0\n\t" \
    "v_mov_b32 v104, 0\n\t" \
    "v_mov_b32 v105, 0\n\t" \
    "v_mov_b32 v106, 0\n\t" \
    "v_mov_b32 v107, 0\n\t" \
    "v_mov_b32 v108, 0\n\t" \
    "v_mov_b32 v109, 0\n\t" \
    "v_mov_b32 v110, 0\n\t" \
    "v_mov_b32 v111, 0\n\t" \
    "v_mov_b32 v112, 0\n\t" \
    "v_mov_b32 v113, 0\n\t" \
    "v_mov_b32 v114, 0\n\t" \
    "v_mov_b32 v115, 0\n\t" \
    "v_mov_b32 v116, 0\n\t" \
    "v_mov_b32 v117, 0\n\t" \
    "v_mov_b32 v118, 0\n\t" \
    "v_mov_b32 v119, 0\n\t" \
    "v_mov_b32 v120, 0\n\t" \
    "v_mov_b32 v121, 0\n\t" \
    "v_mov_b32 v122, 0\n\t" \
    "v_mov_b32 v123, 0\n\t" \
    "v_mov_b32 v124, 0\n\t" \
    "v_mov_b32 v125, 0\n\t" \
    "v_mov_b32 v126, 0\n\t" \
    "v_mov_b32 v127, 0\n\t" \
    "s_mov_b32 %[slot], 0\n\t" \
    "s_mov_b32 %[cnt], 16\n\t" \
    "v_add_u32 v176, %[slot], %[xa0]\n\t" \
    "v_add_u32 v177, %[slot], %[xa1]\n\t" \
    "v_add_u32 v178, %[slot], %[xa2]\n\t" \
    "v_add_u32 v179, %[slot], %[xa3]\n\t" \
    "v_add_u32 v180, %[slot], %[wa0]\n\t" \
    "s_barrier\n\t" \
    "ds_read_b64_tr_b16 v[144:145], v180 offset:0\n\t" \
    "ds_read_b64_tr_b16 v[146:147], v180 offset:512\n\t" \
    "ds_read_b128 v[128:131], v176 offset:0\n\t" \
    "ds_read_b64_tr_b16 v[148:149], v180 offset:8192\n\t" \
    "ds_read_b64_tr_b16 v[150:151], v180 offset:8704\n\t" \
    "ds_read_b128 v[132:135], v176 offset:4096\n\t" \
    "ds_read_b128 v[136:139], v176 offset:8192\n\t" \
    "ds_read_b128 v[140:143], v176 offset:12288\n\t" \
    ".p2align 6\n\t" \
    "1:\n\t" \
    "s_waitcnt lgkmcnt(0)\n\t" \
    "v_mfma_f32_32x32x16_bf16 v[0:15], v[128:131], v[144:147], v[0:15]\n\t" \
    "ds_read_b64_tr_b16 v[168:169], v180 offset:2048\n\t" \
    "ds_read_b64_tr_b16 v[170:171], v180 offset:2560\n\t" \
    "v_mfma_f32_32x32x16_bf16 v[16:31], v[128:131], v[148:151], v[16:31]\n\t" \
    "ds_read_b128 v[152:155], v177 offset:0\n\t" \
    "ds_read_b64_tr_b16 v[172:173], v180 offset:10240\n\t" \
    "v_mfma_f32_32x32x16_bf16 v[32:47], v[132:135], v[144:147], v[32:47]\n\t" \
    "ds_read_b64_tr_b16 v[174:175], v180 offset:10752\n\t" \
    "ds_read_b128 v[156:159], v177 offset:4096\n\t" \
    "v_mfma_f32_32x32x16_bf16 v[48:63], v[132:135], v[148:151], v[48:63]\n\t" \
    "ds_read_b128 v[160:163], v177 offset:8192\n\t" \
    "ds_read_b128 v[164:167], v177 offset:12288\n\t" \
    "v_mfma_f32_32x32x16_bf16 v[64:79], v[136:139], v[144:147], v[64:79]\n\t" \
    "v_mfma_f32_32x32x16_bf16 v[80:95], v[136:139], v[148:151], v[80:95]\n\t" \
    "v_mfma_f32_32x32x16_bf16 v[96:111], v[140:143], v[144:147], v[96:111]\n\t" \
    "v_mfma_f32_32x32x16_bf16 v[112:127], v[140:143], v[148:151], v[112:127]\n\t" \
    "s_waitcnt lgkmcnt(0)\n\t" \
    "v_mfma_f32_32x32x16_bf16 v[0:15], v[152:155], v[168:171], v[0:15]\n\t" \
    "ds_read_b64_tr_b16 v[144:145], v180 offset:4096\n\t" \
    "ds_read_b64_tr_b16 v[146:147], v180 offset:4608\n\t" \
    "v_mfma_f32_32x32x16_bf16 v[16:31], v[152:155], v[172:175], v[16:31]\n\t" \
    "ds_read_b128 v[128:131], v178 offset:0\n\t" \
    "ds_read_b64_tr_b16 v[148:149], v180 offset:12288\n\t" \
    "v_mfma_f32_32x32x16_bf16 v[32:47], v[156:159], v[168:171], v[32:47]\n\t" \
    "ds_read_b64_tr_b16 v[150:151], v180 offset:12800\n\t" \
    "ds_read_b128 v[132:135], v178 offset:4096\n\t" \
    "v_mfma_f32_32x32x16_bf16 v[48:63], v[156:159], v[172:175], v[48:63]\n\t" \
    "ds_read_b128 v[136:139], v178 offset:8192\n\t" \
    "ds_read_b128 v[140:143], v178 offset:12288\n\t" \
    "v_mfma_f32_32x32x16_bf16 v[64:79], v[160:163], v[168:171], v[64:79]\n\t" \
    "v_mfma_f32_32x32x16_bf16 v[80:95], v[160:163], v[172:175], v[80:95]\n\t" \
    "v_mfma_f32_32x32x16_bf16 v[96:111], v[164:167], v[168:171], v[96:111]\n\t" \
    "v_mfma_f32_32x32x16_bf16 v[112:127], v[164:167], v[172:175], v[112:127]\n\t" \
    "s_waitcnt lgkmcnt(0)\n\t" \
    "v_mfma_f32_32x32x16_bf16 v[0:15], v[128:131], v[144:147], v[0:15]\n\t" \
    "ds_read_b64_tr_b16 v[168:169], v180 offset:6144\n\t" \
    "ds_read_b64_tr_b16 v[170:171], v180 offset:6656\n\t" \
    "v_mfma_f32_32x32x16_bf16 v[16:31], v[128:131], v[148:151], v[16:31]\n\t" \
    "ds_read_b128 v[152:155], v179 offset:0\n\t" \
    "ds_read_b64_tr_b16 v[172:173], v180 offset:14336\n\t" \
    "v_mfma_f32_32x32x16_bf16 v[32:47], v[132:135], v[144:147], v[32:47]\n\t" \
    "ds_read_b64_tr_b16 v[174:175], v180 offset:14848\n\t" \
    "ds_read_b128 v[156:159], v179 offset:4096\n\t" \
    "v_mfma_f32_32x32x16_bf16 v[48:63], v[132:135], v[148:151], v[48:63]\n\t" \
    "ds_read_b128 v[160:163], v179 offset:8192\n\t" \
    "ds_read_b128 v[164:167], v179 offset:12288\n\t" \
    "v_mfma_f32_32x32x16_bf16 v[64:79], v[136:139], v[144:147], v[64:79]\n\t" \
    "v_mfma_f32_32x32x16_bf16 v[80:95], v[136:139], v[148:151], v[80:95]\n\t" \
    "v_mfma_f32_32x32x16_bf16 v[96:111], v[140:143], v[144:147], v[96:111]\n\t" \
    "v_mfma_f32_32x32x16_bf16 v[112:127], v[140:143], v[148:151], v[112:127]\n\t" \
    "s_waitcnt lgkmcnt(0)\n\t" \
    "v_mfma_f32_32x32x16_bf16 v[0:15], v[152:155], v[168:171], v[0:15]\n\t" \
    "s_add_u32 %[slot], %[slot], 0xc000\n\t" \
    "s_cmp_eq_u32 %[slot], 0x24000\n\t" \
    "s_cselect_b32 %[slot], 0, %[slot]\n\t" \
    "v_add_u32 v176, %[slot], %[xa0]\n\t" \
    "v_add_u32 v177, %[slot], %[xa1]\n\t" \
    "v_add_u32 v178, %[slot], %[xa2]\n\t" \
    "v_add_u32 v179, %[slot], %[xa3]\n\t" \
    "v_add_u32 v180, %[slot], %[wa0]\n\t" \
    "s_barrier\n\t" \
    "ds_read_b64_tr_b16 v[144:145], v180 offset:0\n\t" \
    "ds_read_b64_tr_b16 v[146:147], v180 offset:512\n\t" \
    "v_mfma_f32_32x32x16_bf16 v[16:31], v[152:155], v[172:175], v[16:31]\n\t" \
    "ds_read_b128 v[128:131], v176 offset:0\n\t" \
    "ds_read_b64_tr_b16 v[148:149], v180 offset:8192\n\t" \
    "v_mfma_f32_32x32x16_bf16 v[32:47], v[156:159], v[168:171], v[32:47]\n\t" \
    "ds_read_b64_tr_b16 v[150:151], v180 offset:8704\n\t" \
    "ds_read_b128 v[132:135], v176 offset:4096\n\t" \
    "v_mfma_f32_32x32x16_bf16 v[48:63], v[156:159], v[172:175], v[48:63]\n\t" \
    "ds_read_b128 v[136:139], v176 offset:8192\n\t" \
    "ds_read_b128 v[140:143], v176 offset:12288\n\t" \
    "v_mfma_f32_32x32x16_bf16 v[64:79], v[160:163], v[168:171], v[64:79]\n\t" \
    "v_mfma_f32_32x32x16_bf16 v[80:95], v[160:163], v[172:175], v[80:95]\n\t" \
    "v_mfma_f32_32x32x16_bf16 v[96:111], v[164:167], v[168:171], v[96:111]\n\t" \
    "v_mfma_f32_32x32x16_bf16 v[112:127], v[164:167], v[172:175], v[112:127]\n\t" \
    "s_sub_u32 %[cnt], %[cnt], 1\n\t" \
    "s_cmp_lg_u32 %[cnt], 0\n\t" \
    "s_cbranch_scc1 1b\n\t" \
    "s_waitcnt lgkmcnt(0)\n\t" \
    "s_nop 7\n\t" \
    "s_nop 7\n\t"
#define G1KV_CLOB "v128", "v129", "v130", "v131", "v132", "v133", "v134", "v135", "v136", "v137", "v138", "v139", "v140", "v141", "v142", "v143", "v144", "v145", "v146", "v147", "v148", "v149", "v150", "v151", "v152", "v153", "v154", "v155", "v156", "v157", "v158", "v159", "v160", "v161", "v162", "v163", "v164", "v165", "v166", "v167", "v168", "v169", "v170", "v171", "v172", "v173", "v174", "v175", "v176", "v177", "v178", "v179", "v180", "v181", "v182", "v183", "v184", "v185", "v186"
#define G1Q_ASM \
    "v_mov_b32 v0, 0\n\t" \
    "v_mov_b32 v1, 0\n\t" \
    "v_mov_b32 v2, 0\n\t" \
    "v_mov_b32 v3, 0\n\t" \
    "v_mov_b32 v4, 0\n\t" \
    "v_mov_b32 v5, 0\n\t" \
    "v_mov_b32 v6, 0\n\t" \
    "v_mov_b32 v7, 0\n\t" \
    "v_mov_b32 v8, 0\n\t" \
    "v_mov_b32 v9, 0\n\t" \
    "v_mov_b32 v10, 0\n\t" \
    "v_mov_b32 v11, 0\n\t" \
    "v_mov_b32 v12, 0\n\t" \
    "v_mov_b32 v13, 0\n\t" \
    "v_mov_b32 v14, 0\n\t" \
    "v_mov_b32 v15, 0\n\t" \
    "v_mov_b32 v16, 0\n\t" \
    "v_mov_b32 v17, 0\n\t" \
    "v_mov_b32 v18, 0\n\t" \
    "v_mov_b32 v19, 0\n\t" \
    "v_mov_b32 v20, 0\n\t" \
    "v_mov_b32 v21, 0\n\t" \
    "v_mov_b32 v22, 0\n\t" \
    "v_mov_b32 v23, 0\n\t" \
    "v_mov_b32 v24, 0\n\t" \
    "v_mov_b32 v25, 0\n\t" \
    "v_mov_b32 v26, 0\n\t" \
    "v_mov_b32 v27, 0\n\t" \
    "v_mov_b32 v28, 0\n\t" \
    "v_mov_b32 v29, 0\n\t" \
    "v_mov_b32 v30, 0\n\t" \
    "v_mov_b32 v31, 0\n\t" \
    "v_mov_b32 v32, 0\n\t" \
    "v_mov_b32 v33, 0\n\t" \
    "v_mov_b32 v34, 0\n\t" \
    "v_mov_b32 v35, 0\n\t" \
    "v_mov_b32 v36, 0\n\t" \
    "v_mov_b32 v37, 0\n\t" \
    "v_mov_b32 v38, 0\n\t" \
    "v_mov_b32 v39, 0\n\t" \
    "v_mov_b32 v40, 0\n\t" \
    "v_mov_b32 v41, 0\n\t" \
    "v_mov_b32 v42, 0\n\t" \
    "v_mov_b32 v43, 0\n\t" \
    "v_mov_b32 v44, 0\n\t" \
    "v_mov_b32 v45, 0\n\t" \
    "v_mov_b32 v46, 0\n\t" \
    "v_mov_b32 v47, 0\n\t" \
    "v_mov_b32 v48, 0\n\t" \
    "v_mov_b32 v49, 0\n\t" \
    "v_mov_b32 v50, 0\n\t" \
    "v_mov_b32 v51, 0\n\t" \
    "v_mov_b32 v52, 0\n\t" \
    "v_mov_b32 v53, 0\n\t" \
    "v_mov_b32 v54, 0\n\t" \
    "v_mov_b32 v55, 0\n\t" \
    "v_mov_b32 v56, 0\n\t" \
    "v_mov_b32 v57, 0\n\t" \
    "v_mov_b32 v58, 0\n\t" \
    "v_mov_b32 v59, 0\n\t" \
    "v_mov_b32 v60, 0\n\t" \
    "v_mov_b32 v61, 0\n\t" \
    "v_mov_b32 v62, 0\n\t" \
    "v_mov_b32 v63, 0\n\t" \
    "v_mov_b32 v64, 0\n\t" \
    "v_mov_b32 v65, 0\n\t" \
    "v_mov_b32 v66, 0\n\t" \
    "v_mov_b32 v67, 0\n\t" \
    "v_mov_b32 v68, 0\n\t" \
    "v_mov_b32 v69, 0\n\t" \
    "v_mov_b32 v70, 0\n\t" \
    "v_mov_b32 v71, 0\n\t" \
    "v_mov_b32 v72, 0\n\t" \
    "v_mov_b32 v73, 0\n\t" \
    "v_mov_b32 v74, 0\n\t" \
    "v_mov_b32 v75, 0\n\t" \
    "v_mov_b32 v76, 0\n\t" \
    "v_mov_b32 v77, 0\n\t" \
    "v_mov_b32 v78, 0\n\t" \
    "v_mov_b32 v79, 0\n\t" \
    "v_mov_b32 v80, 0\n\t" \
    "v_mov_b32 v81, 0\n\t" \
    "v_mov_b32 v82, 0\n\t" \
    "v_mov_b32 v83, 0\n\t" \
    "v_mov_b32 v84, 0\n\t" \
    "v_mov_b32 v85, 0\n\t" \
    "v_mov_b32 v86, 0\n\t" \
    "v_mov_b32 v87, 0\n\t" \
    "v_mov_b32 v88, 0\n\t" \
    "v_mov_b32 v89, 0\n\t" \
    "v_mov_b32 v90, 0\n\t" \
    "v_mov_b32 v91, 0\n\t" \
    "v_mov_b32 v92, 0\n\t" \
    "v_mov_b32 v93, 0\n\t" \
    "v_mov_b32 v94, 0\n\t" \
    "v_mov_b32 v95, 0\n\t" \
    "v_mov_b32 v96, 0\n\t" \
    "v_mov_b32 v97, 0\n\t" \
    "v_mov_b32 v98, 0\n\t" \
    "v_mov_b32 v99, 0\n\t" \
    "v_mov_b32 v100, 0\n\t" \
    "v_mov_b32 v101, 0\n\t" \
    "v_mov_b32 v102, 0\n\t" \
    "v_mov_b32 v103, 0\n\t" \
    "v_mov_b32 v104, 0\n\t" \
    "v_mov_b32 v105, 0\n\t" \
    "v_mov_b32 v106, 0\n\t" \
    "v_mov_b32 v107, 0\n\t" \
    "v_mov_b32 v108, 0\n\t" \
    "v_mov_b32 v109, 0\n\t" \
    "v_mov_b32 v110, 0\n\t" \
    "v_mov_b32 v111, 0\n\t" \
    "v_mov_b32 v112, 0\n\t" \
    "v_mov_b32 v113, 0\n\t" \
    "v_mov_b32 v114, 0\n\t" \
    "v_mov_b32 v115, 0\n\t" \
    "v_mov_b32 v116, 0\n\t" \
    "v_mov_b32 v117, 0\n\t" \
    "v_mov_b32 v118, 0\n\t" \
    "v_mov_b32 v119, 0\n\t" \
    "v_mov_b32 v120, 0\n\t" \
    "v_mov_b32 v121, 0\n\t" \
    "v_mov_b32 v122, 0\n\t" \
    "v_mov_b32 v123, 0\n\t" \
    "v_mov_b32 v124, 0\n\t" \
    "v_mov_b32 v125, 0\n\t" \
    "v_mov_b32 v126, 0\n\t" \
    "v_mov_b32 v127, 0\n\t" \
    "s_mov_b32 %[slot], 0\n\t" \
    "s_mov_b32 %[cnt], 16\n\t" \
    "v_add_u32 v176, %[slot], %[xa0]\n\t" \
    "v_add_u32 v177, %[slot], %[xa1]\n\t" \
    "v_add_u32 v178, %[slot], %[xa2]\n\t" \
    "v_add_u32 v179, %[slot], %[xa3]\n\t" \
    "v_add_u32 v180, %[slot], %[wa0]\n\t" \
    "s_barrier\n\t" \
    "ds_read_b64_tr_b16 v[144:145], v180 offset:0\n\t" \
    "ds_read_b64_tr_b16 v[146:147], v180 offset:512\n\t" \
    "ds_read_b128 v[128:131], v176 offset:0\n\t" \
    "ds_read_b64_tr_b16 v[148:149], v180 offset:8192\n\t" \
    "ds_read_b64_tr_b16 v[150:151], v180 offset:8704\n\t" \
    "ds_read_b128 v[132:135], v176 offset:4096\n\t" \
    "ds_read_b128 v[136:139], v176 offset:8192\n\t" \
    "ds_read_b128 v[140:143], v176 offset:12288\n\t" \
    ".p2align 6\n\t" \
    "1:\n\t" \
    "s_waitcnt lgkmcnt(0)\n\t" \
    "v_mfma_f32_32x32x16_bf16 v[0:15], v[144:147], v[128:131], v[0:15]\n\t" \
    "ds_read_b64_tr_b16 v[168:169], v180 offset:2048\n\t" \
    "ds_read_b64_tr_b16 v[170:171], v180 offset:2560\n\t" \
    "v_mfma_f32_32x32x16_bf16 v[16:31], v[148:151], v[128:131], v[16:31]\n\t" \
    "ds_read_b128 v[152:155], v177 offset:0\n\t" \
    "ds_read_b64_tr_b16 v[172:173], v180 offset:10240\n\t" \
    "v_mfma_f32_32x32x16_bf16 v[32:47], v[144:147], v[132:135], v[32:47]\n\t" \
    "ds_read_b64_tr_b16 v[174:175], v180 offset:10752\n\t" \
    "ds_read_b128 v[156:159], v177 offset:4096\n\t" \
    "v_mfma_f32_32x32x16_bf16 v[48:63], v[148:151], v[132:135], v[48:63]\n\t" \
    "ds_read_b128 v[160:163], v177 offset:8192\n\t" \
    "ds_read_b128 v[164:167], v177 offset:12288\n\t" \
    "v_mfma_f32_32x32x16_bf16 v[64:79], v[144:147], v[136:139], v[64:79]\n\t" \
    "v_mfma_f32_32x32x16_bf16 v[80:95], v[148:151], v[136:139], v[80:95]\n\t" \
    "v_mfma_f32_32x32x16_bf16 v[96:111], v[144:147], v[140:143], v[96:111]\n\t" \
    "v_mfma_f32_32x32x16_bf16 v[112:127], v[148:151], v[140:143], v[112:127]\n\t" \
    "s_waitcnt lgkmcnt(0)\n\t" \
    "v_mfma_f32_32x32x16_bf16 v[0:15], v[168:171], v[152:155], v[0:15]\n\t" \
    "ds_read_b64_tr_b16 v[144:145], v180 offset:4096\n\t" \
    "ds_read_b64_tr_b16 v[146:147], v180 offset:4608\n\t" \
    "v_mfma_f32_32x32x16_bf16 v[16:31], v[172:175], v[152:155], v[16:31]\n\t" \
    "ds_read_b128 v[128:131], v178 offset:0\n\t" \
    "ds_read_b64_tr_b16 v[148:149], v180 offset:12288\n\t" \
    "v_mfma_f32_32x32x16_bf16 v[32:47], v[168:171], v[156:159], v[32:47]\n\t" \
    "ds_read_b64_tr_b16 v[150:151], v180 offset:12800\n\t" \
    "ds_read_b128 v[132:135], v178 offset:4096\n\t" \
    "v_mfma_f32_32x32x16_bf16 v[48:63], v[172:175], v[156:159], v[48:63]\n\t" \
    "ds_read_b128 v[136:139], v178 offset:8192\n\t" \
    "ds_read_b128 v[140:143], v178 offset:12288\n\t" \
    "v_mfma_f32_32x32x16_bf16 v[64:79], v[168:171], v[160:163], v[64:79]\n\t" \
    "v_mfma_f32_32x32x16_bf16 v[80:95], v[172:175], v[160:163], v[80:95]\n\t" \
    "v_mfma_f32_32x32x16_bf16 v[96:111], v[168:171], v[164:167], v[96:111]\n\t" \
    "v_mfma_f32_32x32x16_bf16 v[112:127], v[172:175], v[164:167], v[112:127]\n\t" \
    "s_waitcnt lgkmcnt(0)\n\t" \
    "v_mfma_f32_32x32x16_bf16 v[0:15], v[144:147], v[128:131], v[0:15]\n\t" \
    "ds_read_b64_tr_b16 v[168:169], v180 offset:6144\n\t" \
    "ds_read_b64_tr_b16 v[170:171], v180 offset:6656\n\t" \
    "v_mfma_f32_32x32x16_bf16 v[16:31], v[148:151], v[128:131], v[16:31]\n\t" \
    "ds_read_b128 v[152:155], v179 offset:0\n\t" \
    "ds_read_b64_tr_b16 v[172:173], v180 offset:14336\n\t" \
    "v_mfma_f32_32x32x16_bf16 v[32:47], v[144:147], v[132:135], v[32:47]\n\t" \
    "ds_read_b64_tr_b16 v[174:175], v180 offset:14848\n\t" \
    "ds_read_b128 v[156:159], v179 offset:4096\n\t" \
    "v_mfma_f32_32x32x16_bf16 v[48:63], v[148:151], v[132:135], v[48:63]\n\t" \
    "ds_read_b128 v[160:163], v179 offset:8192\n\t" \
    "ds_read_b128 v[164:167], v179 offset:12288\n\t" \
    "v_mfma_f32_32x32x16_bf16 v[64:79], v[144:147], v[136:139], v[64:79]\n\t" \
    "v_mfma_f32_32x32x16_bf16 v[80:95], v[148:151], v[136:139], v[80:95]\n\t" \
    "v_mfma_f32_32x32x16_bf16 v[96:111], v[144:147], v[140:143], v[96:111]\n\t" \
    "v_mfma_f32_32x32x16_bf16 v[112:127], v[148:151], v[140:143], v[112:127]\n\t" \
    "s_waitcnt lgkmcnt(0)\n\t" \
    "v_mfma_f32_32x32x16_bf16 v[0:15], v[168:171], v[152:155], v[0:15]\n\t" \
    "s_add_u32 %[slot], %[slot], 0xc000\n\t" \
    "s_cmp_eq_u32 %[slot], 0x24000\n\t" \
    "s_cselect_b32 %[slot], 0, %[slot]\n\t" \
    "v_add_u32 v176, %[slot], %[xa0]\n\t" \
    "v_add_u32 v177, %[slot], %[xa1]\n\t" \
    "v_add_u32 v178, %[slot], %[xa2]\n\t" \
    "v_add_u32 v179, %[slot], %[xa3]\n\t" \
    "v_add_u32 v180, %[slot], %[wa0]\n\t" \
    "s_barrier\n\t" \
    "ds_read_b64_tr_b16 v[144:145], v180 offset:0\n\t" \
    "ds_read_b64_tr_b16 v[146:147], v180 offset:512\n\t" \
    "v_mfma_f32_32x32x16_bf16 v[16:31], v[172:175], v[152:155], v[16:31]\n\t" \
    "ds_read_b128 v[128:131], v176 offset:0\n\t" \
    "ds_read_b64_tr_b16 v[148:149], v180 offset:8192\n\t" \
    "v_mfma_f32_32x32x16_bf16 v[32:47], v[168:171], v[156:159], v[32:47]\n\t" \
    "ds_read_b64_tr_b16 v[150:151], v180 offset:8704\n\t" \
    "ds_read_b128 v[132:135], v176 offset:4096\n\t" \
    "v_mfma_f32_32x32x16_bf16 v[48:63], v[172:175], v[156:159], v[48:63]\n\t" \
    "ds_read_b128 v[136:139], v176 offset:8192\n\t" \
    "ds_read_b128 v[140:143], v176 offset:12288\n\t" \
    "v_mfma_f32_32x32x16_bf16 v[64:79], v[168:171], v[160:163], v[64:79]\n\t" \
    "v_mfma_f32_32x32x16_bf16 v[80:95], v[172:175], v[160:163], v[80:95]\n\t" \
    "v_mfma_f32_32x32x16_bf16 v[96:111], v[168:171], v[164:167], v[96:111]\n\t" \
    "v_mfma_f32_32x32x16_bf16 v[112:127], v[172:175], v[164:167], v[112:127]\n\t" \
    "s_sub_u32 %[cnt], %[cnt], 1\n\t" \
    "s_cmp_lg_u32 %[cnt], 0\n\t" \
    "s_cbranch_scc1 1b\n\t" \
    "s_waitcnt lgkmcnt(0)\n\t" \
    "s_nop 7\n\t" \
    "s_nop 7\n\t"
#define G1Q_CLOB "v128", "v129", "v130", "v131", "v132", "v133", "v134", "v135", "v136", "v137", "v138", "v139", "v140", "v141", "v142", "v143", "v144", "v145", "v146", "v147", "v148", "v149", "v150", "v151", "v152", "v153", "v154", "v155", "v156", "v157", "v158", "v159", "v160", "v161", "v162", "v163", "v164", "v165", "v166", "v167", "v168", "v169", "v170", "v171", "v172", "v173", "v174", "v175", "v176", "v177", "v178", "v179", "v180", "v181", "v182", "v183", "v184", "v185", "v186"
#define G1QK_ASM \
    "v_mov_b32 v0, 0\n\t" \
    "v_mov_b32 v1, 0\n\t" \
    "v_mov_b32 v2, 0\n\t" \
    "v_mov_b32 v3, 0\n\t" \
    "v_mov_b32 v4, 0\n\t" \
    "v_mov_b32 v5, 0\n\t" \
    "v_mov_b32 v6, 0\n\t" \
    "v_mov_b32 v7, 0\n\t" \
    "v_mov_b32 v8, 0\n\t" \
    "v_mov_b32 v9, 0\n\t" \
    "v_mov_b32 v10, 0\n\t" \
    "v_mov_b32 v11, 0\n\t" \
    "v_mov_b32 v12, 0\n\t" \
    "v_mov_b32 v13, 0\n\t" \
    "v_mov_b32 v14, 0\n\t" \
    "v_mov_b32 v15, 0\n\t" \
    "v_mov_b32 v16, 0\n\t" \
    "v_mov_b32 v17, 0\n\t" \
    "v_mov_b32 v18, 0\n\t" \
    "v_mov_b32 v19, 0\n\t" \
    "v_mov_b32 v20, 0\n\t" \
    "v_mov_b32 v21, 0\n\t" \
    "v_mov_b32 v22, 0\n\t" \
    "v_mov_b32 v23, 0\n\t" \
    "v_mov_b32 v24, 0\n\t" \
    "v_mov_b32 v25, 0\n\t" \
    "v_mov_b32 v26, 0\n\t" \
    "v_mov_b32 v27, 0\n\t" \
    "v_mov_b32 v28, 0\n\t" \
    "v_mov_b32 v29, 0\n\t" \
    "v_mov_b32 v30, 0\n\t" \
    "v_mov_b32 v31, 0\n\t" \
    "v_mov_b32 v32, 0\n\t" \
    "v_mov_b32 v33, 0\n\t" \
    "v_mov_b32 v34, 0\n\t" \
    "v_mov_b32 v35, 0\n\t" \
    "v_mov_b32 v36, 0\n\t" \
    "v_mov_b32 v37, 0\n\t" \
    "v_mov_b32 v38, 0\n\t" \
    "v_mov_b32 v39, 0\n\t" \
    "v_mov_b32 v40, 0\n\t" \
    "v_mov_b32 v41, 0\n\t" \
    "v_mov_b32 v42, 0\n\t" \
    "v_mov_b32 v43, 0\n\t" \
    "v_mov_b32 v44, 0\n\t" \
    "v_mov_b32 v45, 0\n\t" \
    "v_mov_b32 v46, 0\n\t" \
    "v_mov_b32 v47, 0\n\t" \
    "v_mov_b32 v48, 0\n\t" \
    "v_mov_b32 v49, 0\n\t" \
    "v_mov_b32 v50, 0\n\t" \
    "v_mov_b32 v51, 0\n\t" \
    "v_mov_b32 v52, 0\n\t" \
    "v_mov_b32 v53, 0\n\t" \
    "v_mov_b32 v54, 0\n\t" \
    "v_mov_b32 v55, 0\n\t" \
    "v_mov_b32 v56, 0\n\t" \
    "v_mov_b32 v57, 0\n\t" \
    "v_mov_b32 v58, 0\n\t" \
    "v_mov_b32 v59, 0\n\t" \
    "v_mov_b32 v60, 0\n\t" \
    "v_mov_b32 v61, 0\n\t" \
    "v_mov_b32 v62, 0\n\t" \
    "v_mov_b32 v63, 0\n\t" \
    "v_mov_b32 v64, 0\n\t" \
    "v_mov_b32 v65, 0\n\t" \
    "v_mov_b32 v66, 0\n\t" \
    "v_mov_b32 v67, 0\n\t" \
    "v_mov_b32 v68, 0\n\t" \
    "v_mov_b32 v69, 0\n\t" \
    "v_mov_b32 v70, 0\n\t" \
    "v_mov_b32 v71, 0\n\t" \
    "v_mov_b32 v72, 0\n\t" \
    "v_mov_b32 v73, 0\n\t" \
    "v_mov_b32 v74, 0\n\t" \
    "v_mov_b32 v75, 0\n\t" \
    "v_mov_b32 v76, 0\n\t" \
    "v_mov_b32 v77, 0\n\t" \
    "v_mov_b32 v78, 0\n\t" \
    "v_mov_b32 v79, 0\n\t" \
    "v_mov_b32 v80, 0\n\t" \
    "v_mov_b32 v81, 0\n\t" \
    "v_mov_b32 v82, 0\n\t" \
    "v_mov_b32 v83, 0\n\t" \
    "v_mov_b32 v84, 0\n\t" \
    "v_mov_b32 v85, 0\n\t" \
    "v_mov_b32 v86, 0\n\t" \
    "v_mov_b32 v87, 0\n\t" \
    "v_mov_b32 v88, 0\n\t" \
    "v_mov_b32 v89, 0\n\t" \
    "v_mov_b32 v90, 0\n\t" \
    "v_mov_b32 v91, 0\n\t" \
    "v_mov_b32 v92, 0\n\t" \
    "v_mov_b32 v93, 0\n\t" \
    "v_mov_b32 v94, 0\n\t" \
    "v_mov_b32 v95, 0\n\t" \
    "v_mov_b32 v96, 0\n\t" \
    "v_mov_b32 v97, 0\n\t" \
    "v_mov_b32 v98, 0\n\t" \
    "v_mov_b32 v99, 0\n\t" \
    "v_mov_b32 v100, 0\n\t" \
    "v_mov_b32 v101, 0\n\t" \
    "v_mov_b32 v102, 0\n\t" \
    "v_mov_b32 v103, 0\n\t" \
    "v_mov_b32 v104, 0\n\t" \
    "v_mov_b32 v105, 0\n\t" \
    "v_mov_b32 v106, 0\n\t" \
    "v_mov_b32 v107, 0\n\t" \
    "v_mov_b32 v108, 0\n\t" \
    "v_mov_b32 v109, 0\n\t" \
    "v_mov_b32 v110, 0\n\t" \
    "v_mov_b32 v111, 0\n\t" \
    "v_mov_b32 v112, 0\n\t" \
    "v_mov_b32 v113, 0\n\t" \
    "v_mov_b32 v114, 0\n\t" \
    "v_mov_b32 v115, 0\n\t" \
    "v_mov_b32 v116, 0\n\t" \
    "v_mov_b32 v117, 0\n\t" \
    "v_mov_b32 v118, 0\n\t" \
    "v_mov_b32 v119, 0\n\t" \
    "v_mov_b32 v120, 0\n\t" \
    "v_mov_b32 v121, 0\n\t" \
    "v_mov_b32 v122, 0\n\t" \
    "v_mov_b32 v123, 0\n\t" \
    "v_mov_b32 v124, 0\n\t" \
    "v_mov_b32 v125, 0\n\t" \
    "v_mov_b32 v126, 0\n\t" \
    "v_mov_b32 v127, 0\n\t" \
    "v_mov_b32 v128, 0\n\t" \
    "v_mov_b32 v129, 0\n\t" \
    "v_mov_b32 v130, 0\n\t" \
    "v_mov_b32 v131, 0\n\t" \
    "v_mov_b32 v132, 0\n\t" \
    "v_mov_b32 v133, 0\n\t" \
    "v_mov_b32 v134, 0\n\t" \
    "v_mov_b32 v135, 0\n\t" \
    "v_mov_b32 v136, 0\n\t" \
    "v_mov_b32 v137, 0\n\t" \
    "v_mov_b32 v138, 0\n\t" \
    "v_mov_b32 v139, 0\n\t" \
    "v_mov_b32 v140, 0\n\t" \
    "v_mov_b32 v141, 0\n\t" \
    "v_mov_b32 v142, 0\n\t" \
    "v_mov_b32 v143, 0\n\t" \
    "s_mov_b32 %[slot], 0\n\t" \
    "s_mov_b32 %[cnt], 16\n\t" \
    "v_xor_b32 v214, 0x2000, %[ea]\n\t" \
    "v_add_u32 v208, %[slot], %[xa0]\n\t" \
    "v_add_u32 v209, %[slot], %[xa1]\n\t" \
    "v_add_u32 v210, %[slot], %[xa2]\n\t" \
    "v_add_u32 v211, %[slot], %[xa3]\n\t" \
    "v_add_u32 v212, %[slot], %[wa0]\n\t" \
    "v_xor_b32 v214, 0x2000, v214\n\t" \
    "v_add_u32 v215, %[xe], v208\n\t" \
    "v_add_u32 v216, %[xe], v209\n\t" \
    "v_add_u32 v217, %[xe], v210\n\t" \
    "v_add_u32 v218, %[xe], v211\n\t" \
    "s_barrier\n\t" \
    "ds_read_b64_tr_b16 v[160:161], v212 offset:0\n\t" \
    "ds_read_b64_tr_b16 v[162:163], v212 offset:512\n\t" \
    "ds_read_b128 v[144:147], v208 offset:0\n\t" \
    "ds_read_b64_tr_b16 v[164:165], v212 offset:8192\n\t" \
    "ds_read_b64_tr_b16 v[166:167], v212 offset:8704\n\t" \
    "ds_read_b128 v[148:151], v208 offset:4096\n\t" \
    "ds_read_b128 v[152:155], v208 offset:8192\n\t" \
    "ds_read_b128 v[156:159], v208 offset:12288\n\t" \
    "ds_read_b64_tr_b16 v[172:173], v214 offset:0\n\t" \
    "ds_read_b64_tr_b16 v[174:175], v214 offset:256\n\t" \
    "ds_read_b128 v[168:171], v215\n\t" \
    ".p2align 6\n\t" \
    "1:\n\t" \
    "s_waitcnt lgkmcnt(0)\n\t" \
    "v_mfma_f32_32x32x16_bf16 v[0:15], v[160:163], v[144:147], v[0:15]\n\t" \
    "ds_read_b64_tr_b16 v[192:193], v212 offset:2048\n\t" \
    "ds_read_b64_tr_b16 v[194:195], v212 offset:2560\n\t" \
    "v_mfma_f32_32x32x16_bf16 v[16:31], v[164:167], v[144:147], v[16:31]\n\t" \
    "ds_read_b128 v[176:179], v209 offset:0\n\t" \
    "ds_read_b64_tr_b16 v[196:197], v212 offset:10240\n\t" \
    "v_mfma_f32_32x32x16_bf16 v[32:47], v[160:163], v[148:151], v[32:47]\n\t" \
    "ds_read_b64_tr_b16 v[198:199], v212 offset:10752\n\t" \
    "ds_read_b128 v[180:183], v209 offset:4096\n\t" \
    "v_mfma_f32_32x32x16_bf16 v[48:63], v[164:167], v[148:151], v[48:63]\n\t" \
    "ds_read_b128 v[184:187], v209 offset:8192\n\t" \
    "ds_read_b128 v[188:191], v209 offset:12288\n\t" \
    "v_mfma_f32_32x32x16_bf16 v[64:79], v[160:163], v[152:155], v[64:79]\n\t" \
    "ds_read_b64_tr_b16 v[204:205], v214 offset:1024\n\t" \
    "ds_read_b64_tr_b16 v[206:207], v214 offset:1280\n\t" \
    "v_mfma_f32_32x32x16_bf16 v[80:95], v[164:167], v[152:155], v[80:95]\n\t" \
    "ds_read_b128 v[200:203], v216\n\t" \
    "v_mfma_f32_32x32x16_bf16 v[96:111], v[160:163], v[156:159], v[96:111]\n\t" \
    "v_mfma_f32_32x32x16_bf16 v[112:127], v[164:167], v[156:159], v[112:127]\n\t" \
    "v_mfma_f32_32x32x16_bf16 v[128:143], v[172:175], v[168:171], v[128:143]\n\t" \
    "s_waitcnt lgkmcnt(0)\n\t" \
    "v_mfma_f32_32x32x16_bf16 v[0:15], v[192:195], v[176:179], v[0:15]\n\t" \
    "ds_read_b64_tr_b16 v[160:161], v212 offset:4096\n\t" \
    "ds_read_b64_tr_b16 v[162:163], v212 offset:4608\n\t" \
    "v_mfma_f32_32x32x16_bf16 v[16:31], v[196:199], v[176:179], v[16:31]\n\t" \
    "ds_read_b128 v[144:147], v210 offset:0\n\t" \
    "ds_read_b64_tr_b16 v[164:165], v212 offset:12288\n\t" \
    "v_mfma_f32_32x32x16_bf16 v[32:47], v[192:195], v[180:183], v[32:47]\n\t" \
    "ds_read_b64_tr_b16 v[166:167], v212 offset:12800\n\t" \
    "ds_read_b128 v[148:151], v210 offset:4096\n\t" \
    "v_mfma_f32_32x32x16_bf16 v[48:63], v[196:199], v[180:183], v[48:63]\n\t" \
    "ds_read_b128 v[152:155], v210 offset:8192\n\t" \
    "ds_read_b128 v[156:159], v210 offset:12288\n\t" \
    "v_mfma_f32_32x32x16_bf16 v[64:79], v[192:195], v[184:187], v[64:79]\n\t" \
    "ds_read_b64_tr_b16 v[172:173], v214 offset:2048\n\t" \
    "ds_read_b64_tr_b16 v[174:175], v214 offset:2304\n\t" \
    "v_mfma_f32_32x32x16_bf16 v[80:95], v[196:199], v[184:187], v[80:95]\n\t" \
    "ds_read_b128 v[168:171], v217\n\t" \
    "v_mfma_f32_32x32x16_bf16 v[96:111], v[192:195], v[188:191], v[96:111]\n\t" \
    "v_mfma_f32_32x32x16_bf16 v[112:127], v[196:199], v[188:191], v[112:127]\n\t" \
    "v_mfma_f32_32x32x16_bf16 v[128:143], v[204:207], v[200:203], v[128:143]\n\t" \
    "s_waitcnt lgkmcnt(0)\n\t" \
    "v_mfma_f32_32x32x16_bf16 v[0:15], v[160:163], v[144:147], v[0:15]\n\t" \
    "ds_read_b64_tr_b16 v[192:193], v212 offset:6144\n\t" \
    "ds_read_b64_tr_b16 v[194:195], v212 offset:6656\n\t" \
    "v_mfma_f32_32x32x16_bf16 v[16:31], v[164:167], v[144:147], v[16:31]\n\t" \
    "ds_read_b128 v[176:179], v211 offset:0\n\t" \
    "ds_read_b64_tr_b16 v[196:197], v212 offset:14336\n\t" \
    "v_mfma_f32_32x32x16_bf16 v[32:47], v[160:163], v[148:151], v[32:47]\n\t" \
    "ds_read_b64_tr_b16 v[198:199], v212 offset:14848\n\t" \
    "ds_read_b128 v[180:183], v211 offset:4096\n\t" \
    "v_mfma_f32_32x32x16_bf16 v[48:63], v[164:167], v[148:151], v[48:63]\n\t" \
    "ds_read_b128 v[184:187], v211 offset:8192\n\t" \
    "ds_read_b128 v[188:191], v211 offset:12288\n\t" \
    "v_mfma_f32_32x32x16_bf16 v[64:79], v[160:163], v[152:155], v[64:79]\n\t" \
    "ds_read_b64_tr_b16 v[204:205], v214 offset:3072\n\t" \
    "ds_read_b64_tr_b16 v[206:207], v214 offset:3328\n\t" \
    "v_mfma_f32_32x32x16_bf16 v[80:95], v[164:167], v[152:155], v[80:95]\n\t" \
    "ds_read_b128 v[200:203], v218\n\t" \
    "v_mfma_f32_32x32x16_bf16 v[96:111], v[160:163], v[156:159], v[96:111]\n\t" \
    "v_mfma_f32_32x32x16_bf16 v[112:127], v[164:167], v[156:159], v[112:127]\n\t" \
    "v_mfma_f32_32x32x16_bf16 v[128:143], v[172:175], v[168:171], v[128:143]\n\t" \
    "s_waitcnt lgkmcnt(0)\n\t" \
    "v_mfma_f32_32x32x16_bf16 v[0:15], v[192:195], v[176:179], v[0:15]\n\t" \
    "s_add_u32 %[slot], %[slot], 0xc000\n\t" \
    "s_cmp_eq_u32 %[slot], 0x24000\n\t" \
    "s_cselect_b32 %[slot], 0, %[slot]\n\t" \
    "v_add_u32 v208, %[slot], %[xa0]\n\t" \
    "v_add_u32 v209, %[slot], %[xa1]\n\t" \
    "v_add_u32 v210, %[slot], %[xa2]\n\t" \
    "v_add_u32 v211, %[slot], %[xa3]\n\t" \
    "v_add_u32 v212, %[slot], %[wa0]\n\t" \
    "v_xor_b32 v214, 0x2000, v214\n\t" \
    "v_add_u32 v215, %[xe], v208\n\t" \
    "v_add_u32 v216, %[xe], v209\n\t" \
    "v_add_u32 v217, %[xe], v210\n\t" \
    "v_add_u32 v218, %[xe], v211\n\t" \
    "s_barrier\n\t" \
    "ds_read_b64_tr_b16 v[160:161], v212 offset:0\n\t" \
    "ds_read_b64_tr_b16 v[162:163], v212 offset:512\n\t" \
    "v_mfma_f32_32x32x16_bf16 v[16:31], v[196:199], v[176:179], v[16:31]\n\t" \
    "ds_read_b128 v[144:147], v208 offset:0\n\t" \
    "ds_read_b64_tr_b16 v[164:165], v212 offset:8192\n\t" \
    "v_mfma_f32_32x32x16_bf16 v[32:47], v[192:195], v[180:183], v[32:47]\n\t" \
    "ds_read_b64_tr_b16 v[166:167], v212 offset:8704\n\t" \
    "ds_read_b128 v[148:151], v208 offset:4096\n\t" \
    "v_mfma_f32_32x32x16_bf16 v[48:63], v[196:199], v[180:183], v[48:63]\n\t" \
    "ds_read_b128 v[152:155], v208 offset:8192\n\t" \
    "ds_read_b128 v[156:159], v208 offset:12288\n\t" \
    "v_mfma_f32_32x32x16_bf16 v[64:79], v[192:195], v[184:187], v[64:79]\n\t" \
    "ds_read_b64_tr_b16 v[172:173], v214 offset:0\n\t" \
    "ds_read_b64_tr_b16 v[174:175], v214 offset:256\n\t" \
    "v_mfma_f32_32x32x16_bf16 v[80:95], v[196:199], v[184:187], v[80:95]\n\t" \
    "ds_read_b128 v[168:171], v215\n\t" \
    "v_mfma_f32_32x32x16_bf16 v[96:111], v[192:195], v[188:191], v[96:111]\n\t" \
    "v_mfma_f32_32x32x16_bf16 v[112:127], v[196:199], v[188:191], v[112:127]\n\t" \
    "v_mfma_f32_32x32x16_bf16 v[128:143], v[204:207], v[200:203], v[128:143]\n\t" \
    "s_sub_u32 %[cnt], %[cnt], 1\n\t" \
    "s_cmp_lg_u32 %[cnt], 0\n\t" \
    "s_cbranch_scc1 1b\n\t" \
    "s_waitcnt lgkmcnt(0)\n\t" \
    "s_nop 7\n\t" \
    "s_nop 7\n\t"
#define G1QK_CLOB "v144", "v145", "v146", "v147", "v148", "v149", "v150", "v151", "v152", "v153", "v154", "v155", "v156", "v157", "v158", "v159", "v160", "v161", "v162", "v163", "v164", "v165", "v166", "v167", "v168", "v169", "v170", "v171", "v172", "v173", "v174", "v175", "v176", "v177", "v178", "v179", "v180", "v181", "v182", "v183", "v184", "v185", "v186", "v187", "v188", "v189", "v190", "v191", "v192", "v193", "v194", "v195", "v196", "v197", "v198", "v199", "v200", "v201", "v202", "v203", "v204", "v205", "v206", "v207", "v208", "v209", "v210", "v211", "v212", "v213", "v214", "v215", "v216", "v217", "v218"
#define G1QV_ASM \
    "v_mov_b32 v0, 0\n\t" \
    "v_mov_b32 v1, 0\n\t" \
    "v_mov_b32 v2, 0\n\t" \
    "v_mov_b32 v3, 0\n\t" \
    "v_mov_b32 v4, 0\n\t" \
    "v_mov_b32 v5, 0\n\t" \
    "v_mov_b32 v6, 0\n\t" \
    "v_mov_b32 v7, 0\n\t" \
    "v_mov_b32 v8, 0\n\t" \
    "v_mov_b32 v9, 0\n\t" \
    "v_mov_b32 v10, 0\n\t" \
    "v_mov_b32 v11, 0\n\t" \
    "v_mov_b32 v12, 0\n\t" \
    "v_mov_b32 v13, 0\n\t" \
    "v_mov_b32 v14, 0\n\t" \
    "v_mov_b32 v15, 0\n\t" \
    "v_mov_b32 v16, 0\n\t" \
    "v_mov_b32 v17, 0\n\t" \
    "v_mov_b32 v18, 0\n\t" \
    "v_mov_b32 v19, 0\n\t" \
    "v_mov_b32 v20, 0\n\t" \
    "v_mov_b32 v21, 0\n\t" \
    "v_mov_b32 v22, 0\n\t" \
    "v_mov_b32 v23, 0\n\t" \
    "v_mov_b32 v24, 0\n\t" \
    "v_mov_b32 v25, 0\n\t" \
    "v_mov_b32 v26, 0\n\t" \
    "v_mov_b32 v27, 0\n\t" \
    "v_mov_b32 v28, 0\n\t" \
    "v_mov_b32 v29, 0\n\t" \
    "v_mov_b32 v30, 0\n\t" \
    "v_mov_b32 v31, 0\n\t" \
    "v_mov_b32 v32, 0\n\t" \
    "v_mov_b32 v33, 0\n\t" \
    "v_mov_b32 v34, 0\n\t" \
    "v_mov_b32 v35, 0\n\t" \
    "v_mov_b32 v36, 0\n\t" \
    "v_mov_b32 v37, 0\n\t" \
    "v_mov_b32 v38, 0\n\t" \
    "v_mov_b32 v39, 0\n\t" \
    "v_mov_b32 v40, 0\n\t" \
    "v_mov_b32 v41, 0\n\t" \
    "v_mov_b32 v42, 0\n\t" \
    "v_mov_b32 v43, 0\n\t" \
    "v_mov_b32 v44, 0\n\t" \
    "v_mov_b32 v45, 0\n\t" \
    "v_mov_b32 v46, 0\n\t" \
    "v_mov_b32 v47, 0\n\t" \
    "v_mov_b32 v48, 0\n\t" \
    "v_mov_b32 v49, 0\n\t" \
    "v_mov_b32 v50, 0\n\t" \
    "v_mov_b32 v51, 0\n\t" \
    "v_mov_b32 v52, 0\n\t" \
    "v_mov_b32 v53, 0\n\t" \
    "v_mov_b32 v54, 0\n\t" \
    "v_mov_b32 v55, 0\n\t" \
    "v_mov_b32 v56, 0\n\t" \
    "v_mov_b32 v57, 0\n\t" \
    "v_mov_b32 v58, 0\n\t" \
    "v_mov_b32 v59, 0\n\t" \
    "v_mov_b32 v60, 0\n\t" \
    "v_mov_b32 v61, 0\n\t" \
    "v_mov_b32 v62, 0\n\t" \
    "v_mov_b32 v63, 0\n\t" \
    "v_mov_b32 v64, 0\n\t" \
    "v_mov_b32 v65, 0\n\t" \
    "v_mov_b32 v66, 0\n\t" \
    "v_mov_b32 v67, 0\n\t" \
    "v_mov_b32 v68, 0\n\t" \
    "v_mov_b32 v69, 0\n\t" \
    "v_mov_b32 v70, 0\n\t" \
    "v_mov_b32 v71, 0\n\t" \
    "v_mov_b32 v72, 0\n\t" \
    "v_mov_b32 v73, 0\n\t" \
    "v_mov_b32 v74, 0\n\t" \
    "v_mov_b32 v75, 0\n\t" \
    "v_mov_b32 v76, 0\n\t" \
    "v_mov_b32 v77, 0\n\t" \
    "v_mov_b32 v78, 0\n\t" \
    "v_mov_b32 v79, 0\n\t" \
    "v_mov_b32 v80, 0\n\t" \
    "v_mov_b32 v81, 0\n\t" \
    "v_mov_b32 v82, 0\n\t" \
    "v_mov_b32 v83, 0\n\t" \
    "v_mov_b32 v84, 0\n\t" \
    "v_mov_b32 v85, 0\n\t" \
    "v_mov_b32 v86, 0\n\t" \
    "v_mov_b32 v87, 0\n\t" \
    "v_mov_b32 v88, 0\n\t" \
    "v_mov_b32 v89, 0\n\t" \
    "v_mov_b32 v90, 0\n\t" \
    "v_mov_b32 v91, 0\n\t" \
    "v_mov_b32 v92, 0\n\t" \
    "v_mov_b32 v93, 0\n\t" \
    "v_mov_b32 v94, 0\n\t" \
    "v_mov_b32 v95, 0\n\t" \
    "v_mov_b32 v96, 0\n\t" \
    "v_mov_b32 v97, 0\n\t" \
    "v_mov_b32 v98, 0\n\t" \
    "v_mov_b32 v99, 0\n\t" \
    "v_mov_b32 v100, 0\n\t" \
    "v_mov_b32 v101, 0\n\t" \
    "v_mov_b32 v102, 0\n\t" \
    "v_mov_b32 v103, 0\n\t" \
    "v_mov_b32 v104, 0\n\t" \
    "v_mov_b32 v105, 0\n\t" \
    "v_mov_b32 v106, 0\n\t" \
    "v_mov_b32 v107, 0\n\t" \
    "v_mov_b32 v108, 0\n\t" \
    "v_mov_b32 v109, 0\n\t" \
    "v_mov_b32 v110, 0\n\t" \
    "v_mov_b32 v111, 0\n\t" \
    "v_mov_b32 v112, 0\n\t" \
    "v_mov_b32 v113, 0\n\t" \
    "v_mov_b32 v114, 0\n\t" \
    "v_mov_b32 v115, 0\n\t" \
    "v_mov_b32 v116, 0\n\t" \
    "v_mov_b32 v117, 0\n\t" \
    "v_mov_b32 v118, 0\n\t" \
    "v_mov_b32 v119, 0\n\t" \
    "v_mov_b32 v120, 0\n\t" \
    "v_mov_b32 v121, 0\n\t" \
    "v_mov_b32 v122, 0\n\t" \
    "v_mov_b32 v123, 0\n\t" \
    "v_mov_b32 v124, 0\n\t" \
    "v_mov_b32 v125, 0\n\t" \
    "v_mov_b32 v126, 0\n\t" \
    "v_mov_b32 v127, 0\n\t" \
    "v_mov_b32 v128, 0\n\t" \
    "v_mov_b32 v129, 0\n\t" \
    "v_mov_b32 v130, 0\n\t" \
    "v_mov_b32 v131, 0\n\t" \
    "v_mov_b32 v132, 0\n\t" \
    "v_mov_b32 v133, 0\n\t" \
    "v_mov_b32 v134, 0\n\t" \
    "v_mov_b32 v135, 0\n\t" \
    "v_mov_b32 v136, 0\n\t" \
    "v_mov_b32 v137, 0\n\t" \
    "v_mov_b32 v138, 0\n\t" \
    "v_mov_b32 v139, 0\n\t" \
    "v_mov_b32 v140, 0\n\t" \
    "v_mov_b32 v141, 0\n\t" \
    "v_mov_b32 v142, 0\n\t" \
    "v_mov_b32 v143, 0\n\t" \
    "s_mov_b32 %[slot], 0\n\t" \
    "s_mov_b32 %[cnt], 16\n\t" \
    "v_xor_b32 v214, 0x2000, %[ea]\n\t" \
    "v_add_u32 v208, %[slot], %[xa0]\n\t" \
    "v_add_u32 v209, %[slot], %[xa1]\n\t" \
    "v_add_u32 v210, %[slot], %[xa2]\n\t" \
    "v_add_u32 v211, %[slot], %[xa3]\n\t" \
    "v_add_u32 v212, %[slot], %[wa0]\n\t" \
    "v_xor_b32 v214, 0x2000, v214\n\t" \
    "v_add_u32 v215, %[xe], v208\n\t" \
    "v_add_u32 v216, %[xe], v209\n\t" \
    "v_add_u32 v217, %[xe], v210\n\t" \
    "v_add_u32 v218, %[xe], v211\n\t" \
    "s_barrier\n\t" \
    "ds_read_b64_tr_b16 v[160:161], v212 offset:0\n\t" \
    "ds_read_b64_tr_b16 v[162:163], v212 offset:512\n\t" \
    "ds_read_b128 v[144:147], v208 offset:0\n\t" \
    "ds_read_b64_tr_b16 v[164:165], v212 offset:8192\n\t" \
    "ds_read_b64_tr_b16 v[166:167], v212 offset:8704\n\t" \
    "ds_read_b128 v[148:151], v208 offset:4096\n\t" \
    "ds_read_b128 v[152:155], v208 offset:8192\n\t" \
    "ds_read_b128 v[156:159], v208 offset:12288\n\t" \
    "ds_read_b64_tr_b16 v[172:173], v214 offset:0\n\t" \
    "ds_read_b64_tr_b16 v[174:175], v214 offset:256\n\t" \
    "ds_read_b128 v[168:171], v215\n\t" \
    ".p2align 6\n\t" \
    "1:\n\t" \
    "s_waitcnt lgkmcnt(0)\n\t" \
    "v_mfma_f32_32x32x16_bf16 v[0:15], v[160:163], v[144:147], v[0:15]\n\t" \
    "ds_read_b64_tr_b16 v[192:193], v212 offset:2048\n\t" \
    "ds_read_b64_tr_b16 v[194:195], v212 offset:2560\n\t" \
    "v_mfma_f32_32x32x16_bf16 v[16:31], v[164:167], v[144:147], v[16:31]\n\t" \
    "ds_read_b128 v[176:179], v209 offset:0\n\t" \
    "ds_read_b64_tr_b16 v[196:197], v212 offset:10240\n\t" \
    "v_mfma_f32_32x32x16_bf16 v[32:47], v[160:163], v[148:151], v[32:47]\n\t" \
    "ds_read_b64_tr_b16 v[198:199], v212 offset:10752\n\t" \
    "ds_read_b128 v[180:183], v209 offset:4096\n\t" \
    "v_mfma_f32_32x32x16_bf16 v[48:63], v[164:167], v[148:151], v[48:63]\n\t" \
    "ds_read_b128 v[184:187], v209 offset:8192\n\t" \
    "ds_read_b128 v[188:191], v209 offset:12288\n\t" \
    "v_mfma_f32_32x32x16_bf16 v[64:79], v[160:163], v[152:155], v[64:79]\n\t" \
    "ds_read_b64_tr_b16 v[204:205], v214 offset:1024\n\t" \
    "ds_read_b64_tr_b16 v[206:207], v214 offset:1280\n\t" \
    "v_mfma_f32_32x32x16_bf16 v[80:95], v[164:167], v[152:155], v[80:95]\n\t" \
    "ds_read_b128 v[200:203], v216\n\t" \
    "v_mfma_f32_32x32x16_bf16 v[96:111], v[160:163], v[156:159], v[96:111]\n\t" \
    "v_mfma_f32_32x32x16_bf16 v[112:127], v[164:167], v[156:159], v[112:127]\n\t" \
    "v_mfma_f32_32x32x16_bf16 v[128:143], v[168:171], v[172:175], v[128:143]\n\t" \
    "s_waitcnt lgkmcnt(0)\n\t" \
    "v_mfma_f32_32x32x16_bf16 v[0:15], v[192:195], v[176:179], v[0:15]\n\t" \
    "ds_read_b64_tr_b16 v[160:161], v212 offset:4096\n\t" \
    "ds_read_b64_tr_b16 v[162:163], v212 offset:4608\n\t" \
    "v_mfma_f32_32x32x16_bf16 v[16:31], v[196:199], v[176:179], v[16:31]\n\t" \
    "ds_read_b128 v[144:147], v210 offset:0\n\t" \
    "ds_read_b64_tr_b16 v[164:165], v212 offset:12288\n\t" \
    "v_mfma_f32_32x32x16_bf16 v[32:47], v[192:195], v[180:183], v[32:47]\n\t" \
    "ds_read_b64_tr_b16 v[166:167], v212 offset:12800\n\t" \
    "ds_read_b128 v[148:151], v210 offset:4096\n\t" \
    "v_mfma_f32_32x32x16_bf16 v[48:63], v[196:199], v[180:183], v[48:63]\n\t" \
    "ds_read_b128 v[152:155], v210 offset:8192\n\t" \
    "ds_read_b128 v[156:159], v210 offset:12288\n\t" \
    "v_mfma_f32_32x32x16_bf16 v[64:79], v[192:195], v[184:187], v[64:79]\n\t" \
    "ds_read_b64_tr_b16 v[172:173], v214 offset:2048\n\t" \
    "ds_read_b64_tr_b16 v[174:175], v214 offset:2304\n\t" \
    "v_mfma_f32_32x32x16_bf16 v[80:95], v[196:199], v[184:187], v[80:95]\n\t" \
    "ds_read_b128 v[168:171], v217\n\t" \
    "v_mfma_f32_32x32x16_bf16 v[96:111], v[192:195], v[188:191], v[96:111]\n\t" \
    "v_mfma_f32_32x32x16_bf16 v[112:127], v[196:199], v[188:191], v[112:127]\n\t" \
    "v_mfma_f32_32x32x16_bf16 v[128:143], v[200:203], v[204:207], v[128:143]\n\t" \
    "s_waitcnt lgkmcnt(0)\n\t" \
    "v_mfma_f32_32x32x16_bf16 v[0:15], v[160:163], v[144:147], v[0:15]\n\t" \
    "ds_read_b64_tr_b16 v[192:193], v212 offset:6144\n\t" \
    "ds_read_b64_tr_b16 v[194:195], v212 offset:6656\n\t" \
    "v_mfma_f32_32x32x16_bf16 v[16:31], v[164:167], v[144:147], v[16:31]\n\t" \
    "ds_read_b128 v[176:179], v211 offset:0\n\t" \
    "ds_read_b64_tr_b16 v[196:197], v212 offset:14336\n\t" \
    "v_mfma_f32_32x32x16_bf16 v[32:47], v[160:163], v[148:151], v[32:47]\n\t" \
    "ds_read_b64_tr_b16 v[198:199], v212 offset:14848\n\t" \
    "ds_read_b128 v[180:183], v211 offset:4096\n\t" \
    "v_mfma_f32_32x32x16_bf16 v[48:63], v[164:167], v[148:151], v[48:63]\n\t" \
    "ds_read_b128 v[184:187], v211 offset:8192\n\t" \
    "ds_read_b128 v[188:191], v211 offset:12288\n\t" \
    "v_mfma_f32_32x32x16_bf16 v[64:79], v[160:163], v[152:155], v[64:79]\n\t" \
    "ds_read_b64_tr_b16 v[204:205], v214 offset:3072\n\t" \
    "ds_read_b64_tr_b16 v[206:207], v214 offset:3328\n\t" \
    "v_mfma_f32_32x32x16_bf16 v[80:95], v[164:167], v[152:155], v[80:95]\n\t" \
    "ds_read_b128 v[200:203], v218\n\t" \
    "v_mfma_f32_32x32x16_bf16 v[96:111], v[160:163], v[156:159], v[96:111]\n\t" \
    "v_mfma_f32_32x32x16_bf16 v[112:127], v[164:167], v[156:159], v[112:127]\n\t" \
    "v_mfma_f32_32x32x16_bf16 v[128:143], v[168:171], v[172:175], v[128:143]\n\t" \
    "s_waitcnt lgkmcnt(0)\n\t" \
    "v_mfma_f32_32x32x16_bf16 v[0:15], v[192:195], v[176:179], v[0:15]\n\t" \
    "s_add_u32 %[slot], %[slot], 0xc000\n\t" \
    "s_cmp_eq_u32 %[slot], 0x24000\n\t" \
    "s_cselect_b32 %[slot], 0, %[slot]\n\t" \
    "v_add_u32 v208, %[slot], %[xa0]\n\t" \
    "v_add_u32 v209, %[slot], %[xa1]\n\t" \
    "v_add_u32 v210, %[slot], %[xa2]\n\t" \
    "v_add_u32 v211, %[slot], %[xa3]\n\t" \
    "v_add_u32 v212, %[slot], %[wa0]\n\t" \
    "v_xor_b32 v214, 0x2000, v214\n\t" \
    "v_add_u32 v215, %[xe], v208\n\t" \
    "v_add_u32 v216, %[xe], v209\n\t" \
    "v_add_u32 v217, %[xe], v210\n\t" \
    "v_add_u32 v218, %[xe], v211\n\t" \
    "s_barrier\n\t" \
    "ds_read_b64_tr_b16 v[160:161], v212 offset:0\n\t" \
    "ds_read_b64_tr_b16 v[162:163], v212 offset:512\n\t" \
    "v_mfma_f32_32x32x16_bf16 v[16:31], v[196:199], v[176:179], v[16:31]\n\t" \
    "ds_read_b128 v[144:147], v208 offset:0\n\t" \
    "ds_read_b64_tr_b16 v[164:165], v212 offset:8192\n\t" \
    "v_mfma_f32_32x32x16_bf16 v[32:47], v[192:195], v[180:183], v[32:47]\n\t" \
    "ds_read_b64_tr_b16 v[166:167], v212 offset:8704\n\t" \
    "ds_read_b128 v[148:151], v208 offset:4096\n\t" \
    "v_mfma_f32_32x32x16_bf16 v[48:63], v[196:199], v[180:183], v[48:63]\n\t" \
    "ds_read_b128 v[152:155], v208 offset:8192\n\t" \
    "ds_read_b128 v[156:159], v208 offset:12288\n\t" \
    "v_mfma_f32_32x32x16_bf16 v[64:79], v[192:195], v[184:187], v[64:79]\n\t" \
    "ds_read_b64_tr_b16 v[172:173], v214 offset:0\n\t" \
    "ds_read_b64_tr_b16 v[174:175], v214 offset:256\n\t" \
    "v_mfma_f32_32x32x16_bf16 v[80:95], v[196:199], v[184:187], v[80:95]\n\t" \
    "ds_read_b128 v[168:171], v215\n\t" \
    "v_mfma_f32_32x32x16_bf16 v[96:111], v[192:195], v[188:191], v[96:111]\n\t" \
    "v_mfma_f32_32x32x16_bf16 v[112:127], v[196:199], v[188:191], v[112:127]\n\t" \
    "v_mfma_f32_32x32x16_bf16 v[128:143], v[200:203], v[204:207], v[128:143]\n\t" \
    "s_sub_u32 %[cnt], %[cnt], 1\n\t" \
    "s_cmp_lg_u32 %[cnt], 0\n\t" \
    "s_cbranch_scc1 1b\n\t" \
    "s_waitcnt lgkmcnt(0)\n\t" \
    "s_nop 7\n\t" \
    "s_nop 7\n\t"
#define G1QV_CLOB "v144", "v145", "v146", "v147", "v148", "v149", "v150", "v151", "v152", "v153", "v154", "v155", "v156", "v157", "v158", "v159", "v160", "v161", "v162", "v163", "v164", "v165", "v166", "v167", "v168", "v169", "v170", "v171", "v172", "v173", "v174", "v175", "v176", "v177", "v178", "v179", "v180", "v181", "v182", "v183", "v184", "v185", "v186", "v187", "v188", "v189", "v190", "v191", "v192", "v193", "v194", "v195", "v196", "v197", "v198", "v199", "v200", "v201", "v202", "v203", "v204", "v205", "v206", "v207", "v208", "v209", "v210", "v211", "v212", "v213", "v214", "v215", "v216", "v217", "v218"
#define G3_ASM \
    "v_mov_b32 v0, 0\n\t" \
    "v_mov_b32 v1, 0\n\t" \
    "v_mov_b32 v2, 0\n\t" \
    "v_mov_b32 v3, 0\n\t" \
    "v_mov_b32 v4, 0\n\t" \
    "v_mov_b32 v5, 0\n\t" \
    "v_mov_b32 v6, 0\n\t" \
    "v_mov_b32 v7, 0\n\t" \
    "v_mov_b32 v8, 0\n\t" \
    "v_mov_b32 v9, 0\n\t" \
    "v_mov_b32 v10, 0\n\t" \
    "v_mov_b32 v11, 0\n\t" \
    "v_mov_b32 v12, 0\n\t" \
    "v_mov_b32 v13, 0\n\t" \
    "v_mov_b32 v14, 0\n\t" \
    "v_mov_b32 v15, 0\n\t" \
    "v_mov_b32 v16, 0\n\t" \
    "v_mov_b32 v17, 0\n\t" \
    "v_mov_b32 v18, 0\n\t" \
    "v_mov_b32 v19, 0\n\t" \
    "v_mov_b32 v20, 0\n\t" \
    "v_mov_b32 v21, 0\n\t" \
    "v_mov_b32 v22, 0\n\t" \
    "v_mov_b32 v23, 0\n\t" \
    "v_mov_b32 v24, 0\n\t" \
    "v_mov_b32 v25, 0\n\t" \
    "v_mov_b32 v26, 0\n\t" \
    "v_mov_b32 v27, 0\n\t" \
    "v_mov_b32 v28, 0\n\t" \
    "v_mov_b32 v29, 0\n\t" \
    "v_mov_b32 v30, 0\n\t" \
    "v_mov_b32 v31, 0\n\t" \
    "v_mov_b32 v32, 0\n\t" \
    "v_mov_b32 v33, 0\n\t" \
    "v_mov_b32 v34, 0\n\t" \
    "v_mov_b32 v35, 0\n\t" \
    "v_mov_b32 v36, 0\n\t" \
    "v_mov_b32 v37, 0\n\t" \
    "v_mov_b32 v38, 0\n\t" \
    "v_mov_b32 v39, 0\n\t" \
    "v_mov_b32 v40, 0\n\t" \
    "v_mov_b32 v41, 0\n\t" \
    "v_mov_b32 v42, 0\n\t" \
    "v_mov_b32 v43, 0\n\t" \
    "v_mov_b32 v44, 0\n\t" \
    "v_mov_b32 v45, 0\n\t" \
    "v_mov_b32 v46, 0\n\t" \
    "v_mov_b32 v47, 0\n\t" \
    "v_mov_b32 v48, 0\n\t" \
    "v_mov_b32 v49, 0\n\t" \
    "v_mov_b32 v50, 0\n\t" \
    "v_mov_b32 v51, 0\n\t" \
    "v_mov_b32 v52, 0\n\t" \
    "v_mov_b32 v53, 0\n\t" \
    "v_mov_b32 v54, 0\n\t" \
    "v_mov_b32 v55, 0\n\t" \
    "v_mov_b32 v56, 0\n\t" \
    "v_mov_b32 v57, 0\n\t" \
    "v_mov_b32 v58, 0\n\t" \
    "v_mov_b32 v59, 0\n\t" \
    "v_mov_b32 v60, 0\n\t" \
    "v_mov_b32 v61, 0\n\t" \
    "v_mov_b32 v62, 0\n\t" \
    "v_mov_b32 v63, 0\n\t" \
    "s_mov_b32 %[slot], 0\n\t" \
    "s_mov_b32 %[cnt], 16\n\t" \
    "v_add_u32 v96, %[slot], %[xa0]\n\t" \
    "v_add_u32 v100, %[slot], %[wa0]\n\t" \
    "v_add_u32 v101, %[slot], %[wa1]\n\t" \
    "s_barrier\n\t" \
    "ds_read_b64_tr_b16 v[72:73], v100 offset:0\n\t" \
    "ds_read_b64_tr_b16 v[74:75], v100 offset:1024\n\t" \
    "ds_read_b128 v[64:67], v96 offset:0\n\t" \
    "ds_read_b64_tr_b16 v[76:77], v101 offset:0\n\t" \
    "ds_read_b64_tr_b16 v[78:79], v101 offset:1024\n\t" \
    "ds_read_b128 v[68:71], v96 offset:4096\n\t" \
    ".p2align 6\n\t" \
    "1:\n\t" \
    "s_waitcnt lgkmcnt(0)\n\t" \
    "v_mfma_f32_32x32x16_bf16 v[0:15], v[72:75], v[64:67], v[0:15]\n\t" \
    "ds_read_b64_tr_b16 v[88:89], v100 offset:2048\n\t" \
    "ds_read_b64_tr_b16 v[90:91], v100 offset:3072\n\t" \
    "v_mfma_f32_32x32x16_bf16 v[16:31], v[76:79], v[64:67], v[16:31]\n\t" \
    "ds_read_b128 v[80:83], v96 offset:1024\n\t" \
    "ds_read_b64_tr_b16 v[92:93], v101 offset:2048\n\t" \
    "v_mfma_f32_32x32x16_bf16 v[32:47], v[72:75], v[68:71], v[32:47]\n\t" \
    "ds_read_b64_tr_b16 v[94:95], v101 offset:3072\n\t" \
    "ds_read_b128 v[84:87], v96 offset:5120\n\t" \
    "v_mfma_f32_32x32x16_bf16 v[48:63], v[76:79], v[68:71], v[48:63]\n\t" \
    "s_waitcnt lgkmcnt(0)\n\t" \
    "v_mfma_f32_32x32x16_bf16 v[0:15], v[88:91], v[80:83], v[0:15]\n\t" \
    "ds_read_b64_tr_b16 v[72:73], v100 offset:4096\n\t" \
    "ds_read_b64_tr_b16 v[74:75], v100 offset:5120\n\t" \
    "v_mfma_f32_32x32x16_bf16 v[16:31], v[92:95], v[80:83], v[16:31]\n\t" \
    "ds_read_b128 v[64:67], v96 offset:2048\n\t" \
    "ds_read_b64_tr_b16 v[76:77], v101 offset:4096\n\t" \
    "v_mfma_f32_32x32x16_bf16 v[32:47], v[88:91], v[84:87], v[32:47]\n\t" \
    "ds_read_b64_tr_b16 v[78:79], v101 offset:5120\n\t" \
    "ds_read_b128 v[68:71], v96 offset:6144\n\t" \
    "v_mfma_f32_32x32x16_bf16 v[48:63], v[92:95], v[84:87], v[48:63]\n\t" \
    "s_waitcnt lgkmcnt(0)\n\t" \
    "v_mfma_f32_32x32x16_bf16 v[0:15], v[72:75], v[64:67], v[0:15]\n\t" \
    "ds_read_b64_tr_b16 v[88:89], v100 offset:6144\n\t" \
    "ds_read_b64_tr_b16 v[90:91], v100 offset:7168\n\t" \
    "v_mfma_f32_32x32x16_bf16 v[16:31], v[76:79], v[64:67], v[16:31]\n\t" \
    "ds_read_b128 v[80:83], v96 offset:3072\n\t" \
    "ds_read_b64_tr_b16 v[92:93], v101 offset:6144\n\t" \
    "v_mfma_f32_32x32x16_bf16 v[32:47], v[72:75], v[68:71], v[32:47]\n\t" \
    "ds_read_b64_tr_b16 v[94:95], v101 offset:7168\n\t" \
    "ds_read_b128 v[84:87], v96 offset:7168\n\t" \
    "v_mfma_f32_32x32x16_bf16 v[48:63], v[76:79], v[68:71], v[48:63]\n\t" \
    "s_waitcnt lgkmcnt(0)\n\t" \
    "v_mfma_f32_32x32x16_bf16 v[0:15], v[88:91], v[80:83], v[0:15]\n\t" \
    "s_add_u32 %[slot], %[slot], 0x8000\n\t" \
    "s_and_b32 %[slot], %[slot], 0x1ffff\n\t" \
    "v_add_u32 v96, %[slot], %[xa0]\n\t" \
    "v_add_u32 v100, %[slot], %[wa0]\n\t" \
    "v_add_u32 v101, %[slot], %[wa1]\n\t" \
    "s_barrier\n\t" \
    "ds_read_b64_tr_b16 v[72:73], v100 offset:0\n\t" \
    "ds_read_b64_tr_b16 v[74:75], v100 offset:1024\n\t" \
    "v_mfma_f32_32x32x16_bf16 v[16:31], v[92:95], v[80:83], v[16:31]\n\t" \
    "ds_read_b128 v[64:67], v96 offset:0\n\t" \
    "ds_read_b64_tr_b16 v[76:77], v101 offset:0\n\t" \
    "v_mfma_f32_32x32x16_bf16 v[32:47], v[88:91], v[84:87], v[32:47]\n\t" \
    "ds_read_b64_tr_b16 v[78:79], v101 offset:1024\n\t" \
    "ds_read_b128 v[68:71], v96 offset:4096\n\t" \
    "v_mfma_f32_32x32x16_bf16 v[48:63], v[92:95], v[84:87], v[48:63]\n\t" \
    "s_sub_u32 %[cnt], %[cnt], 1\n\t" \
    "s_cmp_lg_u32 %[cnt], 0\n\t" \
    "s_cbranch_scc1 1b\n\t" \
    "s_waitcnt lgkmcnt(0)\n\t" \
    "s_nop 7\n\t" \
    "s_nop 7\n\t"
#define G3_CLOB "v64", "v65", "v66", "v67", "v68", "v69", "v70", "v71", "v72", "v73", "v74", "v75", "v76", "v77", "v78", "v79", "v80", "v81", "v82", "v83", "v84", "v85", "v86", "v87", "v88", "v89", "v90", "v91", "v92", "v93", "v94", "v95", "v96", "v97", "v98", "v99", "v100", "v101", "v102", "v103", "v104", "v105", "v106"

#define ACC8_OUT "=&{v[0:15]}"(acc[0][0]), "=&{v[16:31]}"(acc[0][1]), "=&{v[32:47]}"(acc[1][0]), "=&{v[48:63]}"(acc[1][1]), \
                 "=&{v[64:79]}"(acc[2][0]), "=&{v[80:95]}"(acc[2][1]), "=&{v[96:111]}"(acc[3][0]), "=&{v[112:127]}"(acc[3][1])
template <int VAR>
DI void k1_mainloop(unsigned lds0, int wr, int wc, int lane, int xrow, f32x16 (&acc)[4][2], f32x16& accx) {
  const int q = (lane & 15) >> 2, p = lane & 3;
  unsigned slot, cnt;
  const int r = lane & 31, h = lane >> 5, g = (r >> 1) & 7, blk = (lane >> 4) & 1;
  const unsigned xb = lds0 + (wr * 128 + r) * 128;
  const unsigned xa0 = xb + ((h ^ g) << 4), xa1 = xb + (((2 + h) ^ g) << 4), xa2 = xb + (((4 + h) ^ g) << 4), xa3 = xb + (((6 + h) ^ g) << 4);
  const unsigned wa0 = lds0 + K1_XB + (8 * h + q) * 128 + ((wc ^ (q >> 1)) << 6) + blk * 32 + p * 8;
  const unsigned ea = lds0 + K1_KVOFF + (VAR == 3 ? 4096 : 0) + (8 * h + q) * 64 + blk * 32 + p * 8;
  const int xe = (xrow - wr * 128) * 128;
#define G1_IN [xa0] "v"(xa0), [xa1] "v"(xa1), [xa2] "v"(xa2), [xa3] "v"(xa3), [wa0] "v"(wa0)
  if (VAR == 0) asm volatile(G1KV_ASM : ACC8_OUT, [slot] "=&s"(slot), [cnt] "=&s"(cnt) : G1_IN : "memory", "scc", G1KV_CLOB);
  if (VAR == 1) asm volatile(G1Q_ASM : ACC8_OUT, [slot] "=&s"(slot), [cnt] "=&s"(cnt) : G1_IN : "memory", "scc", G1Q_CLOB);
  if (VAR == 2) asm volatile(G1QK_ASM : ACC8_OUT, "=&{v[128:143]}"(accx), [slot] "=&s"(slot), [cnt] "=&s"(cnt) : G1_IN, [ea] "v"(ea), [xe] "s"(xe) : "memory", "scc", G1QK_CLOB);
  if (VAR == 3) asm volatile(G1QV_ASM : ACC8_OUT, "=&{v[128:143]}"(accx), [slot] "=&s"(slot), [cnt] "=&s"(cnt) : G1_IN, [ea] "v"(ea), [xe] "s"(xe) : "memory", "scc", G1QV_CLOB);
}

__global__ __launch_bounds__(512, 2) void k1_proj(const char* __restrict__ xA, const char* __restrict__ wA, const char* __restrict__ xB, const char* __restrict__ wqB,
                                                  const char* __restrict__ wkB, const char* __restrict__ wvB, char* __restrict__ ws) {
  extern __shared__ __attribute__((aligned(16))) char smem[];
  lds_ptr lds = (lds_ptr)smem;
  const int lane = threadIdx.x & 63, wid = rfl(threadIdx.x >> 6);
  const int xcd = blockIdx.x & 7, idx = blockIdx.x >> 3, lr = idx & 3, lc = idx >> 2;
  const int rt = (xcd & 1) * 4 + lr, cg = xcd >> 1;
  const bool isB = cg == 3, isKV = cg < 2;
  STAMP(0, 0);
  if (wid >= 4) {
    const int lw = wid - 4;
    const unsigned lds0 = (unsigned)(uintptr_t)smem;
    const unsigned ldw2 = isB ? 2048 : 6144;
    const unsigned col0 = isKV ? (1024 + (cg * 8 + lc) * 64) * 2 : lc * 256, col1 = isKV ? col0 + 2048 : col0 + 128;
    const __amdgpu_buffer_rsrc_t sx = mk_srd(isB ? xB : xA, 4u << 20), sw = mk_srd(isB ? wqB : wA, isB ? (2u << 20) : (6u << 20));
    const __amdgpu_buffer_rsrc_t se = mk_srd(lw < 2 ? wkB : wvB, 65536);
    const unsigned vx0 = (lane >> 3) * 2048 + (((lane & 7) ^ (lane >> 4)) << 4), vx1 = (lane >> 3) * 2048 + (((lane & 7) ^ (4 + (lane >> 4))) << 4);
    const unsigned vw = (lane >> 3) * ldw2 + (((lane & 7) ^ (((lane >> 4) & 1) << 2)) << 4), ve = lane * 16;
    const unsigned sx0 = (rt * 256 + lw * 64) * 2048, sw0 = (lw & 1) * 32 * ldw2 + ((lw >> 1) ? col1 : col0);
    const unsigned dx = lds0 + lw * 8192, dw = lds0 + K1_XB + lw * 4096, de = lds0 + K1_KVOFF + lw * 2048;
    const int k0 = K0A;
    auto issue = [&](int st, unsigned slot) {
      const unsigned kt = (st + k0) & 15;
#pragma unroll
      for (int i = 0; i < 8; ++i) dma16(dx + slot + i * 1024, (i & 1) ? vx1 : vx0, sx, sx0 + kt * 128 + i * 16384);
#pragma unroll
      for (int i = 0; i < 4; ++i) dma16(dw + slot + i * 1024, vw, sw, sw0 + (kt * 64 + i * 8) * ldw2);
    };
    auto issue_e = [&](int st) {
      const unsigned kt = (st + k0) & 15;
#pragma unroll
      for (int i = 0; i < 2; ++i) dma16(de + (st & 1) * 8192 + i * 1024, ve, se, kt * 4096 + (lw & 1) * 2048 + i * 1024);
    };
    issue(0, 0); if (isB) issue_e(0);
    issue(1, K1_SLOT); if (isB) issue_e(1);
    issue(2, 2 * K1_SLOT);
    if (isB) WAITV(26); else WAITV(24);
    BARRIER();
    unsigned slot = 0;
    for (int st = 1; st < 15; ++st) {
      WAITV(12);
      BARRIER();
      if (isB) issue_e(st + 1);
      if (st < 14) issue(st + 2, slot); else WAITV(0);
      slot = (slot == 2 * K1_SLOT) ? 0 : slot + K1_SLOT;
    }
    BARRIER();
    BARRIER();
    STAMP(0, 3);
    return;
  }
  const int wr = wid >> 1, wc = wid & 1;
  f32x16 acc[4][2], accx;
  const unsigned lds0 = (unsigned)(uintptr_t)smem;
  if (isKV) {
    k1_mainloop<0>(lds0, wr, wc, lane, 0, acc, accx);
    STAMP(0, 2);
    f32x16 kv = {}, zz = {};
    const u32x4 onesw = {0x3f803f80u, 0x3f803f80u, 0x3f803f80u, 0x3f803f80u};
    const bf16x8 ones = __builtin_bit_cast(bf16x8, onesw);
#pragma unroll
    for (int mt = 0; mt < 4; ++mt) {
#pragma unroll
      for (int i = 0; i < 16; ++i) acc[mt][0][i] = phi(acc[mt][0][i]);
#pragma unroll
      for (int s = 0; s < 2; ++s) { const bf16x8 pk = pack8(acc[mt][0], 8 * s); kv = MFMA32(pk, pack8(acc[mt][1], 8 * s), kv); zz = MFMA32(pk, ones, zz); }
    }
    BARRIER();
    __attribute__((address_space(3))) f32x4* ex = (__attribute__((address_space(3))) f32x4*)(lds + wc * 8192) + lane;
    if (wr == 1) {
#pragma unroll
      for (int i = 0; i < 4; ++i) { ex[i * 64] = f32x4{kv[4 * i], kv[4 * i + 1], kv[4 * i + 2], kv[4 * i + 3]}; ex[256 + i * 64] = f32x4{zz[4 * i], zz[4 * i + 1], zz[4 * i + 2], zz[4 * i + 3]}; }
    }
    BARRIER();
    if (wr == 0) {
      const int head = (cg * 8 + lc) * 2 + wc;
      f32x4* kp = (f32x4*)(ws + WS_KVP + (size_t)(head * 8 + rt) * 4096) + lane;
      f32x4* zp = (f32x4*)(ws + WS_ZP + (size_t)(head * 8 + rt) * 128 + (lane >> 5) * 64);
#pragma unroll
      for (int i = 0; i < 4; ++i) {
        const f32x4 o = ex[i * 64], oz = ex[256 + i * 64];
        kp[i * 64] = f32x4{kv[4 * i] + o[0], kv[4 * i + 1] + o[1], kv[4 * i + 2] + o[2], kv[4 * i + 3] + o[3]};
        if ((lane & 31) == 0) zp[i] = f32x4{zz[4 * i] + oz[0], zz[4 * i + 1] + oz[1], zz[4 * i + 2] + oz[2], zz[4 * i + 3] + oz[3]};
      }
    }
    STAMP(0, 3);
    return;
  }
  const bool extra = isB && wr == (lc >> 2);
  if (!extra) k1_mainloop<1>(lds0, wr, wc, lane, 0, acc, accx);
  else if (wc == 0) k1_mainloop<2>(lds0, wr, wc, lane, lc * 32, acc, accx);
  else k1_mainloop<3>(lds0, wr, wc, lane, lc * 32, acc, accx);
  STAMP(0, 2);
  const float sc = isB ? QSCALE : 1.0f;
  char* qo = ws + (isB ? WS_QB : WS_QA) + ((size_t)(rt * 8 + wr * 4) * 32 + lc * 4 + wc) * 2048 + lane * 16;
#pragma unroll
  for (int mt = 0; mt < 4; ++mt)
#pragma unroll
    for (int nt = 0; nt < 2; ++nt) {
      const f32x16 v = acc[mt][nt] * sc;
      *(bf16x8*)(qo + mt * 65536 + nt * 4096) = pack8(v, 0);
      *(bf16x8*)(qo + mt * 65536 + nt * 4096 + 1024) = pack8(v, 8);
    }
  if (extra) {
    const int tt = rt * 8 + lc;
    if (wc == 0) {
      char* ko = ws + WS_KVB + ((tt + 63) & 63) * 4096 + lane * 16;
      *(bf16x8*)ko = pack8(accx, 0);
      *(bf16x8*)(ko + 1024) = pack8(accx, 8);
    } else {
      char* vo = ws + WS_KVB + tt * 4096 + 2048 + (lane & 31) * 16 + (lane >> 5) * 8;
#pragma unroll
      for (int a = 0; a < 4; ++a) *(u32x2*)(vo + a * 512) = u32x2{cvtpk(accx[4 * a], accx[4 * a + 1]), cvtpk(accx[4 * a + 2], accx[4 * a + 3])};
    }
  }
  STAMP(0, 3);
}

constexpr int K2_SLOT = 16384, K2_EX = 8 * K2_SLOT, K2_K0 = K2_EX + 4096, K2_LDS = K2_K0 + 2048;

DI void attn_exact(const char* kvb, bf16x8 q0, bf16x8 q1, int lane, char* dst) {
  f32x16 o = {}; float m = -INFINITY, l = 0.f;
  for (int t = 0; t < 64; ++t) {
    const char* kp = kvb + ((t + 63) & 63) * 4096 + lane * 16; const char* vp = kvb + t * 4096 + 2048 + lane * 16;
    f32x16 s = MFMA32(*(const bf16x8*)kp, q0, f32x16{}); s = MFMA32(*(const bf16x8*)(kp + 1024), q1, s);
    float mx = s[0];
#pragma unroll
    for (int i = 1; i < 16; ++i) mx = fmaxf(mx, s[i]);
    mx = fmaxf(mx, __shfl_xor(mx, 32));
    const float mn = fmaxf(m, mx), al = __builtin_amdgcn_exp2f(m - mn);
    float ps = 0.f;
#pragma unroll
    for (int i = 0; i < 16; ++i) { s[i] = __uint_as_float(__float_as_uint(__builtin_amdgcn_exp2f(s[i] - mn)) & 0xffff0000u); ps += s[i]; }
    ps += __shfl_xor(ps, 32);
    l = l * al + ps; m = mn; o = o * al;
    const bf16x8 v0 = *(const bf16x8*)vp, v1 = *(const bf16x8*)(vp + 1024);
    typedef float f32x8 __attribute__((ext_vector_type(8)));
    const f32x8 s0 = __builtin_shufflevector(s, s, 0, 1, 2, 3, 4, 5, 6, 7), s1 = __builtin_shufflevector(s, s, 8, 9, 10, 11, 12, 13, 14, 15);
    asm volatile("s_nop 1\n\tv_smfmac_f32_32x32x32_bf16 %0, %1, %2, %5\n\tv_smfmac_f32_32x32x32_bf16 %0, %3, %4, %5\n\ts_nop 7\n\ts_nop 7\n\ts_nop 7"
                 : "+v"(o) : "v"(v0), "v"(s0), "v"(v1), "v"(s1), "v"(0xddddddddu));
  }
  o = o * (1.0f / l);
  *(bf16x8*)dst = pack8(o, 0); *(bf16x8*)(dst + 1024) = pack8(o, 8);
}

#define ATTN_ASM \
    "ds_read_b128 v[160:163], %[ka]\n\t" \
    "ds_read_b128 v[164:167], %[ka] offset:1024\n\t" \
    "v_mov_b32 v144, 0\n\t" \
    "v_mov_b32 v145, 0\n\t" \
    "v_mov_b32 v146, 0\n\t" \
    "v_mov_b32 v147, 0\n\t" \
    "v_mov_b32 v148, 0\n\t" \
    "v_mov_b32 v149, 0\n\t" \
    "v_mov_b32 v150, 0\n\t" \
    "v_mov_b32 v151, 0\n\t" \
    "v_mov_b32 v152, 0\n\t" \
    "v_mov_b32 v153, 0\n\t" \
    "v_mov_b32 v154, 0\n\t" \
    "v_mov_b32 v155, 0\n\t" \
    "v_mov_b32 v156, 0\n\t" \
    "v_mov_b32 v157, 0\n\t" \
    "v_mov_b32 v158, 0\n\t" \
    "v_mov_b32 v159, 0\n\t" \
    "v_mov_b32 v184, 0\n\t" \
    "v_mov_b32 v185, 0\n\t" \
    "v_mov_b32 v186, 0\n\t" \
    "v_mov_b32 v187, 0\n\t" \
    "v_mov_b32 v188, 0\n\t" \
    "v_mov_b32 v189, 0\n\t" \
    "v_mov_b32 v190, 0\n\t" \
    "v_mov_b32 v191, 0\n\t" \
    "v_mov_b32 v192, %[lad]\n\t" \
    "s_mov_b32 %[cnt], 0\n\t" \
    "s_waitcnt lgkmcnt(0)\n\t" \
    "v_mfma_f32_32x32x16_bf16 v[96:111], v[160:163], %[qa0], %[na]\n\t" \
    "v_mfma_f32_32x32x16_bf16 v[96:111], v[164:167], %[qa1], v[96:111]\n\t" \
    "s_nop 7\n\t" \
    "s_nop 7\n\t" \
    "s_nop 7\n\t" \
    ".p2align 6\n\t" \
    "1:\n\t" \
    "v_mfma_f32_32x32x16_bf16 v[128:143], v[160:163], %[qb0], %[nb]\n\t" \
    "ds_read_b128 v[168:171], v192 offset:0\n\t" \
    "ds_read_b128 v[172:175], v192 offset:1024\n\t" \
    "v_exp_f32_e32 v96, v96\n\t" \
    "v_exp_f32_e32 v97, v97\n\t" \
    "v_exp_f32_e32 v98, v98\n\t" \
    "v_smfmac_f32_32x32x32_bf16 %[ob], v[184:187], v[144:151], %[idx]\n\t" \
    "v_exp_f32_e32 v99, v99\n\t" \
    "v_exp_f32_e32 v100, v100\n\t" \
    "v_exp_f32_e32 v101, v101\n\t" \
    "v_mfma_f32_32x32x16_bf16 v[128:143], v[164:167], %[qb1], v[128:143]\n\t" \
    "ds_read_b128 v[176:179], v192 offset:2048\n\t" \
    "ds_read_b128 v[180:183], v192 offset:3072\n\t" \
    "v_exp_f32_e32 v102, v102\n\t" \
    "v_exp_f32_e32 v103, v103\n\t" \
    "v_exp_f32_e32 v104, v104\n\t" \
    "v_smfmac_f32_16x16x64_bf16 %[lb], %[ones], v[144:151], %[idx]\n\t" \
    "v_exp_f32_e32 v105, v105\n\t" \
    "v_exp_f32_e32 v106, v106\n\t" \
    "v_smfmac_f32_32x32x32_bf16 %[ob], v[188:191], v[152:159], %[idx]\n\t" \
    "v_exp_f32_e32 v107, v107\n\t" \
    "v_exp_f32_e32 v108, v108\n\t" \
    "v_exp_f32_e32 v109, v109\n\t" \
    "v_smfmac_f32_16x16x64_bf16 %[lb], %[ones], v[152:159], %[idx]\n\t" \
    "v_exp_f32_e32 v110, v110\n\t" \
    "v_exp_f32_e32 v111, v111\n\t" \
    "s_waitcnt lgkmcnt(0)\n\t" \
    "v_mfma_f32_32x32x16_bf16 v[112:127], v[168:171], %[qa0], %[na]\n\t" \
    "v_exp_f32_e32 v128, v128\n\t" \
    "v_exp_f32_e32 v129, v129\n\t" \
    "v_exp_f32_e32 v130, v130\n\t" \
    "v_smfmac_f32_32x32x32_bf16 %[oa], v[176:179], v[96:103], %[idx]\n\t" \
    "v_exp_f32_e32 v131, v131\n\t" \
    "v_exp_f32_e32 v132, v132\n\t" \
    "v_exp_f32_e32 v133, v133\n\t" \
    "v_mfma_f32_32x32x16_bf16 v[112:127], v[172:175], %[qa1], v[112:127]\n\t" \
    "v_exp_f32_e32 v134, v134\n\t" \
    "v_exp_f32_e32 v135, v135\n\t" \
    "v_exp_f32_e32 v136, v136\n\t" \
    "v_smfmac_f32_16x16x64_bf16 %[la], %[ones], v[96:103], %[idx]\n\t" \
    "v_exp_f32_e32 v137, v137\n\t" \
    "v_exp_f32_e32 v138, v138\n\t" \
    "v_smfmac_f32_32x32x32_bf16 %[oa], v[180:183], v[104:111], %[idx]\n\t" \
    "v_exp_f32_e32 v139, v139\n\t" \
    "v_exp_f32_e32 v140, v140\n\t" \
    "v_exp_f32_e32 v141, v141\n\t" \
    "v_smfmac_f32_16x16x64_bf16 %[la], %[ones], v[104:111], %[idx]\n\t" \
    "v_exp_f32_e32 v142, v142\n\t" \
    "v_exp_f32_e32 v143, v143\n\t" \
    "v_mfma_f32_32x32x16_bf16 v[144:159], v[168:171], %[qb0], %[nb]\n\t" \
    "ds_read_b128 v[160:163], v192 offset:4096\n\t" \
    "ds_read_b128 v[164:167], v192 offset:5120\n\t" \
    "v_exp_f32_e32 v112, v112\n\t" \
    "v_exp_f32_e32 v113, v113\n\t" \
    "v_exp_f32_e32 v114, v114\n\t" \
    "v_smfmac_f32_32x32x32_bf16 %[ob], v[176:179], v[128:135], %[idx]\n\t" \
    "v_exp_f32_e32 v115, v115\n\t" \
    "v_exp_f32_e32 v116, v116\n\t" \
    "v_exp_f32_e32 v117, v117\n\t" \
    "v_mfma_f32_32x32x16_bf16 v[144:159], v[172:175], %[qb1], v[144:159]\n\t" \
    "ds_read_b128 v[184:187], v192 offset:6144\n\t" \
    "ds_read_b128 v[188:191], v192 offset:7168\n\t" \
    "v_exp_f32_e32 v118, v118\n\t" \
    "v_exp_f32_e32 v119, v119\n\t" \
    "v_exp_f32_e32 v120, v120\n\t" \
    "v_smfmac_f32_16x16x64_bf16 %[lb], %[ones], v[128:135], %[idx]\n\t" \
    "v_exp_f32_e32 v121, v121\n\t" \
    "v_exp_f32_e32 v122, v122\n\t" \
    "v_smfmac_f32_32x32x32_bf16 %[ob], v[180:183], v[136:143], %[idx]\n\t" \
    "v_exp_f32_e32 v123, v123\n\t" \
    "v_exp_f32_e32 v124, v124\n\t" \
    "v_exp_f32_e32 v125, v125\n\t" \
    "v_smfmac_f32_16x16x64_bf16 %[lb], %[ones], v[136:143], %[idx]\n\t" \
    "v_exp_f32_e32 v126, v126\n\t" \
    "v_exp_f32_e32 v127, v127\n\t" \
    "s_waitcnt lgkmcnt(0)\n\t" \
    "v_mfma_f32_32x32x16_bf16 v[96:111], v[160:163], %[qa0], %[na]\n\t" \
    "v_exp_f32_e32 v144, v144\n\t" \
    "v_exp_f32_e32 v145, v145\n\t" \
    "v_exp_f32_e32 v146, v146\n\t" \
    "v_smfmac_f32_32x32x32_bf16 %[oa], v[184:187], v[112:119], %[idx]\n\t" \
    "v_exp_f32_e32 v147, v147\n\t" \
    "v_exp_f32_e32 v148, v148\n\t" \
    "v_exp_f32_e32 v149, v149\n\t" \
    "v_mfma_f32_32x32x16_bf16 v[96:111], v[164:167], %[qa1], v[96:111]\n\t" \
    "v_exp_f32_e32 v150, v150\n\t" \
    "v_exp_f32_e32 v151, v151\n\t" \
    "v_exp_f32_e32 v152, v152\n\t" \
    "v_smfmac_f32_16x16x64_bf16 %[la], %[ones], v[112:119], %[idx]\n\t" \
    "v_exp_f32_e32 v153, v153\n\t" \
    "v_exp_f32_e32 v154, v154\n\t" \
    "v_smfmac_f32_32x32x32_bf16 %[oa], v[188:191], v[120:127], %[idx]\n\t" \
    "v_exp_f32_e32 v155, v155\n\t" \
    "v_exp_f32_e32 v156, v156\n\t" \
    "v_exp_f32_e32 v157, v157\n\t" \
    "v_smfmac_f32_16x16x64_bf16 %[la], %[ones], v[120:127], %[idx]\n\t" \
    "v_exp_f32_e32 v158, v158\n\t" \
    "v_exp_f32_e32 v159, v159\n\t" \
    "v_mfma_f32_32x32x16_bf16 v[128:143], v[160:163], %[qb0], %[nb]\n\t" \
    "ds_read_b128 v[168:171], v192 offset:8192\n\t" \
    "ds_read_b128 v[172:175], v192 offset:9216\n\t" \
    "v_exp_f32_e32 v96, v96\n\t" \
    "v_exp_f32_e32 v97, v97\n\t" \
    "v_exp_f32_e32 v98, v98\n\t" \
    "v_smfmac_f32_32x32x32_bf16 %[ob], v[184:187], v[144:151], %[idx]\n\t" \
    "v_exp_f32_e32 v99, v99\n\t" \
    "v_exp_f32_e32 v100, v100\n\t" \
    "v_exp_f32_e32 v101, v101\n\t" \
    "v_mfma_f32_32x32x16_bf16 v[128:143], v[164:167], %[qb1], v[128:143]\n\t" \
    "ds_read_b128 v[176:179], v192 offset:10240\n\t" \
    "ds_read_b128 v[180:183], v192 offset:11264\n\t" \
    "v_exp_f32_e32 v102, v102\n\t" \
    "v_exp_f32_e32 v103, v103\n\t" \
    "v_exp_f32_e32 v104, v104\n\t" \
    "v_smfmac_f32_16x16x64_bf16 %[lb], %[ones], v[144:151], %[idx]\n\t" \
    "v_exp_f32_e32 v105, v105\n\t" \
    "v_exp_f32_e32 v106, v106\n\t" \
    "v_smfmac_f32_32x32x32_bf16 %[ob], v[188:191], v[152:159], %[idx]\n\t" \
    "v_exp_f32_e32 v107, v107\n\t" \
    "v_exp_f32_e32 v108, v108\n\t" \
    "v_exp_f32_e32 v109, v109\n\t" \
    "v_smfmac_f32_16x16x64_bf16 %[lb], %[ones], v[152:159], %[idx]\n\t" \
    "v_exp_f32_e32 v110, v110\n\t" \
    "v_exp_f32_e32 v111, v111\n\t" \
    "s_waitcnt lgkmcnt(0)\n\t" \
    "v_mfma_f32_32x32x16_bf16 v[112:127], v[168:171], %[qa0], %[na]\n\t" \
    "v_exp_f32_e32 v128, v128\n\t" \
    "v_exp_f32_e32 v129, v129\n\t" \
    "v_exp_f32_e32 v130, v130\n\t" \
    "v_smfmac_f32_32x32x32_bf16 %[oa], v[176:179], v[96:103], %[idx]\n\t" \
    "v_exp_f32_e32 v131, v131\n\t" \
    "v_exp_f32_e32 v132, v132\n\t" \
    "v_exp_f32_e32 v133, v133\n\t" \
    "v_mfma_f32_32x32x16_bf16 v[112:127], v[172:175], %[qa1], v[112:127]\n\t" \
    "v_exp_f32_e32 v134, v134\n\t" \
    "v_exp_f32_e32 v135, v135\n\t" \
    "v_exp_f32_e32 v136, v136\n\t" \
    "v_smfmac_f32_16x16x64_bf16 %[la], %[ones], v[96:103], %[idx]\n\t" \
    "v_exp_f32_e32 v137, v137\n\t" \
    "v_exp_f32_e32 v138, v138\n\t" \
    "v_smfmac_f32_32x32x32_bf16 %[oa], v[180:183], v[104:111], %[idx]\n\t" \
    "v_exp_f32_e32 v139, v139\n\t" \
    "v_exp_f32_e32 v140, v140\n\t" \
    "v_exp_f32_e32 v141, v141\n\t" \
    "v_smfmac_f32_16x16x64_bf16 %[la], %[ones], v[104:111], %[idx]\n\t" \
    "v_exp_f32_e32 v142, v142\n\t" \
    "v_exp_f32_e32 v143, v143\n\t" \
    "v_mfma_f32_32x32x16_bf16 v[144:159], v[168:171], %[qb0], %[nb]\n\t" \
    "ds_read_b128 v[160:163], v192 offset:12288\n\t" \
    "ds_read_b128 v[164:167], v192 offset:13312\n\t" \
    "v_exp_f32_e32 v112, v112\n\t" \
    "v_exp_f32_e32 v113, v113\n\t" \
    "v_exp_f32_e32 v114, v114\n\t" \
    "v_smfmac_f32_32x32x32_bf16 %[ob], v[176:179], v[128:135], %[idx]\n\t" \
    "v_exp_f32_e32 v115, v115\n\t" \
    "v_exp_f32_e32 v116, v116\n\t" \
    "v_exp_f32_e32 v117, v117\n\t" \
    "v_mfma_f32_32x32x16_bf16 v[144:159], v[172:175], %[qb1], v[144:159]\n\t" \
    "ds_read_b128 v[184:187], v192 offset:14336\n\t" \
    "ds_read_b128 v[188:191], v192 offset:15360\n\t" \
    "v_exp_f32_e32 v118, v118\n\t" \
    "v_exp_f32_e32 v119, v119\n\t" \
    "v_exp_f32_e32 v120, v120\n\t" \
    "v_smfmac_f32_16x16x64_bf16 %[lb], %[ones], v[128:135], %[idx]\n\t" \
    "v_exp_f32_e32 v121, v121\n\t" \
    "v_exp_f32_e32 v122, v122\n\t" \
    "v_smfmac_f32_32x32x32_bf16 %[ob], v[180:183], v[136:143], %[idx]\n\t" \
    "v_exp_f32_e32 v123, v123\n\t" \
    "v_exp_f32_e32 v124, v124\n\t" \
    "v_exp_f32_e32 v125, v125\n\t" \
    "v_smfmac_f32_16x16x64_bf16 %[lb], %[ones], v[136:143], %[idx]\n\t" \
    "v_exp_f32_e32 v126, v126\n\t" \
    "v_exp_f32_e32 v127, v127\n\t" \
    "s_waitcnt lgkmcnt(0)\n\t" \
    "v_mfma_f32_32x32x16_bf16 v[96:111], v[160:163], %[qa0], %[na]\n\t" \
    "v_exp_f32_e32 v144, v144\n\t" \
    "v_exp_f32_e32 v145, v145\n\t" \
    "v_exp_f32_e32 v146, v146\n\t" \
    "v_smfmac_f32_32x32x32_bf16 %[oa], v[184:187], v[112:119], %[idx]\n\t" \
    "v_exp_f32_e32 v147, v147\n\t" \
    "v_exp_f32_e32 v148, v148\n\t" \
    "v_exp_f32_e32 v149, v149\n\t" \
    "v_mfma_f32_32x32x16_bf16 v[96:111], v[164:167], %[qa1], v[96:111]\n\t" \
    "v_exp_f32_e32 v150, v150\n\t" \
    "v_exp_f32_e32 v151, v151\n\t" \
    "v_exp_f32_e32 v152, v152\n\t" \
    "v_smfmac_f32_16x16x64_bf16 %[la], %[ones], v[112:119], %[idx]\n\t" \
    "v_exp_f32_e32 v153, v153\n\t" \
    "v_exp_f32_e32 v154, v154\n\t" \
    "v_smfmac_f32_32x32x32_bf16 %[oa], v[188:191], v[120:127], %[idx]\n\t" \
    "v_exp_f32_e32 v155, v155\n\t" \
    "v_exp_f32_e32 v156, v156\n\t" \
    "v_exp_f32_e32 v157, v157\n\t" \
    "v_smfmac_f32_16x16x64_bf16 %[la], %[ones], v[120:127], %[idx]\n\t" \
    "v_exp_f32_e32 v158, v158\n\t" \
    "v_exp_f32_e32 v159, v159\n\t" \
    "s_add_u32 %[cnt], %[cnt], 1\n\t" \
    "s_bitcmp1_b32 %[bm], %[cnt]\n\t" \
    "s_cbranch_scc0 2f\n\t" \
    "s_waitcnt lgkmcnt(0)\n\t" \
    "s_barrier\n\t" \
    "2:\n\t" \
    "v_mfma_f32_32x32x16_bf16 v[128:143], v[160:163], %[qb0], %[nb]\n\t" \
    "ds_read_b128 v[168:171], v192 offset:16384\n\t" \
    "ds_read_b128 v[172:175], v192 offset:17408\n\t" \
    "v_exp_f32_e32 v96, v96\n\t" \
    "v_exp_f32_e32 v97, v97\n\t" \
    "v_exp_f32_e32 v98, v98\n\t" \
    "v_smfmac_f32_32x32x32_bf16 %[ob], v[184:187], v[144:151], %[idx]\n\t" \
    "v_exp_f32_e32 v99, v99\n\t" \
    "v_exp_f32_e32 v100, v100\n\t" \
    "v_exp_f32_e32 v101, v101\n\t" \
    "v_mfma_f32_32x32x16_bf16 v[128:143], v[164:167], %[qb1], v[128:143]\n\t" \
    "ds_read_b128 v[176:179], v192 offset:18432\n\t" \
    "ds_read_b128 v[180:183], v192 offset:19456\n\t" \
    "v_exp_f32_e32 v102, v102\n\t" \
    "v_exp_f32_e32 v103, v103\n\t" \
    "v_exp_f32_e32 v104, v104\n\t" \
    "v_smfmac_f32_16x16x64_bf16 %[lb], %[ones], v[144:151], %[idx]\n\t" \
    "v_exp_f32_e32 v105, v105\n\t" \
    "v_exp_f32_e32 v106, v106\n\t" \
    "v_smfmac_f32_32x32x32_bf16 %[ob], v[188:191], v[152:159], %[idx]\n\t" \
    "v_exp_f32_e32 v107, v107\n\t" \
    "v_exp_f32_e32 v108, v108\n\t" \
    "v_exp_f32_e32 v109, v109\n\t" \
    "v_smfmac_f32_16x16x64_bf16 %[lb], %[ones], v[152:159], %[idx]\n\t" \
    "v_exp_f32_e32 v110, v110\n\t" \
    "v_exp_f32_e32 v111, v111\n\t" \
    "s_waitcnt lgkmcnt(0)\n\t" \
    "v_mfma_f32_32x32x16_bf16 v[112:127], v[168:171], %[qa0], %[na]\n\t" \
    "v_exp_f32_e32 v128, v128\n\t" \
    "v_exp_f32_e32 v129, v129\n\t" \
    "v_exp_f32_e32 v130, v130\n\t" \
    "v_smfmac_f32_32x32x32_bf16 %[oa], v[176:179], v[96:103], %[idx]\n\t" \
    "v_exp_f32_e32 v131, v131\n\t" \
    "v_exp_f32_e32 v132, v132\n\t" \
    "v_exp_f32_e32 v133, v133\n\t" \
    "v_mfma_f32_32x32x16_bf16 v[112:127], v[172:175], %[qa1], v[112:127]\n\t" \
    "v_exp_f32_e32 v134, v134\n\t" \
    "v_exp_f32_e32 v135, v135\n\t" \
    "v_exp_f32_e32 v136, v136\n\t" \
    "v_smfmac_f32_16x16x64_bf16 %[la], %[ones], v[96:103], %[idx]\n\t" \
    "v_exp_f32_e32 v137, v137\n\t" \
    "v_exp_f32_e32 v138, v138\n\t" \
    "v_smfmac_f32_32x32x32_bf16 %[oa], v[180:183], v[104:111], %[idx]\n\t" \
    "v_exp_f32_e32 v139, v139\n\t" \
    "v_exp_f32_e32 v140, v140\n\t" \
    "v_exp_f32_e32 v141, v141\n\t" \
    "v_smfmac_f32_16x16x64_bf16 %[la], %[ones], v[104:111], %[idx]\n\t" \
    "v_exp_f32_e32 v142, v142\n\t" \
    "v_exp_f32_e32 v143, v143\n\t" \
    "v_mfma_f32_32x32x16_bf16 v[144:159], v[168:171], %[qb0], %[nb]\n\t" \
    "ds_read_b128 v[160:163], v192 offset:20480\n\t" \
    "ds_read_b128 v[164:167], v192 offset:21504\n\t" \
    "v_exp_f32_e32 v112, v112\n\t" \
    "v_exp_f32_e32 v113, v113\n\t" \
    "v_exp_f32_e32 v114, v114\n\t" \
    "v_smfmac_f32_32x32x32_bf16 %[ob], v[176:179], v[128:135], %[idx]\n\t" \
    "v_exp_f32_e32 v115, v115\n\t" \
    "v_exp_f32_e32 v116, v116\n\t" \
    "v_exp_f32_e32 v117, v117\n\t" \
    "v_mfma_f32_32x32x16_bf16 v[144:159], v[172:175], %[qb1], v[144:159]\n\t" \
    "ds_read_b128 v[184:187], v192 offset:22528\n\t" \
    "ds_read_b128 v[188:191], v192 offset:23552\n\t" \
    "v_exp_f32_e32 v118, v118\n\t" \
    "v_exp_f32_e32 v119, v119\n\t" \
    "v_exp_f32_e32 v120, v120\n\t" \
    "v_smfmac_f32_16x16x64_bf16 %[lb], %[ones], v[128:135], %[idx]\n\t" \
    "v_exp_f32_e32 v121, v121\n\t" \
    "v_exp_f32_e32 v122, v122\n\t" \
    "v_smfmac_f32_32x32x32_bf16 %[ob], v[180:183], v[136:143], %[idx]\n\t" \
    "v_exp_f32_e32 v123, v123\n\t" \
    "v_exp_f32_e32 v124, v124\n\t" \
    "v_exp_f32_e32 v125, v125\n\t" \
    "v_smfmac_f32_16x16x64_bf16 %[lb], %[ones], v[136:143], %[idx]\n\t" \
    "v_exp_f32_e32 v126, v126\n\t" \
    "v_exp_f32_e32 v127, v127\n\t" \
    "s_waitcnt lgkmcnt(0)\n\t" \
    "v_mfma_f32_32x32x16_bf16 v[96:111], v[160:163], %[qa0], %[na]\n\t" \
    "v_exp_f32_e32 v144, v144\n\t" \
    "v_exp_f32_e32 v145, v145\n\t" \
    "v_exp_f32_e32 v146, v146\n\t" \
    "v_smfmac_f32_32x32x32_bf16 %[oa], v[184:187], v[112:119], %[idx]\n\t" \
    "v_exp_f32_e32 v147, v147\n\t" \
    "v_exp_f32_e32 v148, v148\n\t" \
    "v_exp_f32_e32 v149, v149\n\t" \
    "v_mfma_f32_32x32x16_bf16 v[96:111], v[164:167], %[qa1], v[96:111]\n\t" \
    "v_exp_f32_e32 v150, v150\n\t" \
    "v_exp_f32_e32 v151, v151\n\t" \
    "v_exp_f32_e32 v152, v152\n\t" \
    "v_smfmac_f32_16x16x64_bf16 %[la], %[ones], v[112:119], %[idx]\n\t" \
    "v_exp_f32_e32 v153, v153\n\t" \
    "v_exp_f32_e32 v154, v154\n\t" \
    "v_smfmac_f32_32x32x32_bf16 %[oa], v[188:191], v[120:127], %[idx]\n\t" \
    "v_exp_f32_e32 v155, v155\n\t" \
    "v_exp_f32_e32 v156, v156\n\t" \
    "v_exp_f32_e32 v157, v157\n\t" \
    "v_smfmac_f32_16x16x64_bf16 %[la], %[ones], v[120:127], %[idx]\n\t" \
    "v_exp_f32_e32 v158, v158\n\t" \
    "v_exp_f32_e32 v159, v159\n\t" \
    "v_mfma_f32_32x32x16_bf16 v[128:143], v[160:163], %[qb0], %[nb]\n\t" \
    "ds_read_b128 v[168:171], v192 offset:24576\n\t" \
    "ds_read_b128 v[172:175], v192 offset:25600\n\t" \
    "v_exp_f32_e32 v96, v96\n\t" \
    "v_exp_f32_e32 v97, v97\n\t" \
    "v_exp_f32_e32 v98, v98\n\t" \
    "v_smfmac_f32_32x32x32_bf16 %[ob], v[184:187], v[144:151], %[idx]\n\t" \
    "v_exp_f32_e32 v99, v99\n\t" \
    "v_exp_f32_e32 v100, v100\n\t" \
    "v_exp_f32_e32 v101, v101\n\t" \
    "v_mfma_f32_32x32x16_bf16 v[128:143], v[164:167], %[qb1], v[128:143]\n\t" \
    "ds_read_b128 v[176:179], v192 offset:26624\n\t" \
    "ds_read_b128 v[180:183], v192 offset:27648\n\t" \
    "v_exp_f32_e32 v102, v102\n\t" \
    "v_exp_f32_e32 v103, v103\n\t" \
    "v_exp_f32_e32 v104, v104\n\t" \
    "v_smfmac_f32_16x16x64_bf16 %[lb], %[ones], v[144:151], %[idx]\n\t" \
    "v_exp_f32_e32 v105, v105\n\t" \
    "v_exp_f32_e32 v106, v106\n\t" \
    "v_smfmac_f32_32x32x32_bf16 %[ob], v[188:191], v[152:159], %[idx]\n\t" \
    "v_exp_f32_e32 v107, v107\n\t" \
    "v_exp_f32_e32 v108, v108\n\t" \
    "v_exp_f32_e32 v109, v109\n\t" \
    "v_smfmac_f32_16x16x64_bf16 %[lb], %[ones], v[152:159], %[idx]\n\t" \
    "v_exp_f32_e32 v110, v110\n\t" \
    "v_exp_f32_e32 v111, v111\n\t" \
    "s_waitcnt lgkmcnt(0)\n\t" \
    "v_mfma_f32_32x32x16_bf16 v[112:127], v[168:171], %[qa0], %[na]\n\t" \
    "v_exp_f32_e32 v128, v128\n\t" \
    "v_exp_f32_e32 v129, v129\n\t" \
    "v_exp_f32_e32 v130, v130\n\t" \
    "v_smfmac_f32_32x32x32_bf16 %[oa], v[176:179], v[96:103], %[idx]\n\t" \
    "v_exp_f32_e32 v131, v131\n\t" \
    "v_exp_f32_e32 v132, v132\n\t" \
    "v_exp_f32_e32 v133, v133\n\t" \
    "v_mfma_f32_32x32x16_bf16 v[112:127], v[172:175], %[qa1], v[112:127]\n\t" \
    "v_exp_f32_e32 v134, v134\n\t" \
    "v_exp_f32_e32 v135, v135\n\t" \
    "v_exp_f32_e32 v136, v136\n\t" \
    "v_smfmac_f32_16x16x64_bf16 %[la], %[ones], v[96:103], %[idx]\n\t" \
    "v_exp_f32_e32 v137, v137\n\t" \
    "v_exp_f32_e32 v138, v138\n\t" \
    "v_smfmac_f32_32x32x32_bf16 %[oa], v[180:183], v[104:111], %[idx]\n\t" \
    "v_exp_f32_e32 v139, v139\n\t" \
    "v_exp_f32_e32 v140, v140\n\t" \
    "v_exp_f32_e32 v141, v141\n\t" \
    "v_smfmac_f32_16x16x64_bf16 %[la], %[ones], v[104:111], %[idx]\n\t" \
    "v_exp_f32_e32 v142, v142\n\t" \
    "v_exp_f32_e32 v143, v143\n\t" \
    "v_mfma_f32_32x32x16_bf16 v[144:159], v[168:171], %[qb0], %[nb]\n\t" \
    "ds_read_b128 v[160:163], v192 offset:28672\n\t" \
    "ds_read_b128 v[164:167], v192 offset:29696\n\t" \
    "v_exp_f32_e32 v112, v112\n\t" \
    "v_exp_f32_e32 v113, v113\n\t" \
    "v_exp_f32_e32 v114, v114\n\t" \
    "v_smfmac_f32_32x32x32_bf16 %[ob], v[176:179], v[128:135], %[idx]\n\t" \
    "v_exp_f32_e32 v115, v115\n\t" \
    "v_exp_f32_e32 v116, v116\n\t" \
    "v_exp_f32_e32 v117, v117\n\t" \
    "v_mfma_f32_32x32x16_bf16 v[144:159], v[172:175], %[qb1], v[144:159]\n\t" \
    "ds_read_b128 v[184:187], v192 offset:30720\n\t" \
    "ds_read_b128 v[188:191], v192 offset:31744\n\t" \
    "v_exp_f32_e32 v118, v118\n\t" \
    "v_exp_f32_e32 v119, v119\n\t" \
    "v_exp_f32_e32 v120, v120\n\t" \
    "v_smfmac_f32_16x16x64_bf16 %[lb], %[ones], v[128:135], %[idx]\n\t" \
    "v_exp_f32_e32 v121, v121\n\t" \
    "v_exp_f32_e32 v122, v122\n\t" \
    "v_smfmac_f32_32x32x32_bf16 %[ob], v[180:183], v[136:143], %[idx]\n\t" \
    "v_exp_f32_e32 v123, v123\n\t" \
    "v_exp_f32_e32 v124, v124\n\t" \
    "v_exp_f32_e32 v125, v125\n\t" \
    "v_smfmac_f32_16x16x64_bf16 %[lb], %[ones], v[136:143], %[idx]\n\t" \
    "v_exp_f32_e32 v126, v126\n\t" \
    "v_exp_f32_e32 v127, v127\n\t" \
    "s_waitcnt lgkmcnt(0)\n\t" \
    "v_mfma_f32_32x32x16_bf16 v[96:111], v[160:163], %[qa0], %[na]\n\t" \
    "v_exp_f32_e32 v144, v144\n\t" \
    "v_exp_f32_e32 v145, v145\n\t" \
    "v_exp_f32_e32 v146, v146\n\t" \
    "v_smfmac_f32_32x32x32_bf16 %[oa], v[184:187], v[112:119], %[idx]\n\t" \
    "v_exp_f32_e32 v147, v147\n\t" \
    "v_exp_f32_e32 v148, v148\n\t" \
    "v_exp_f32_e32 v149, v149\n\t" \
    "v_mfma_f32_32x32x16_bf16 v[96:111], v[164:167], %[qa1], v[96:111]\n\t" \
    "v_exp_f32_e32 v150, v150\n\t" \
    "v_exp_f32_e32 v151, v151\n\t" \
    "v_exp_f32_e32 v152, v152\n\t" \
    "v_smfmac_f32_16x16x64_bf16 %[la], %[ones], v[112:119], %[idx]\n\t" \
    "v_exp_f32_e32 v153, v153\n\t" \
    "v_exp_f32_e32 v154, v154\n\t" \
    "v_smfmac_f32_32x32x32_bf16 %[oa], v[188:191], v[120:127], %[idx]\n\t" \
    "v_exp_f32_e32 v155, v155\n\t" \
    "v_exp_f32_e32 v156, v156\n\t" \
    "v_exp_f32_e32 v157, v157\n\t" \
    "v_smfmac_f32_16x16x64_bf16 %[la], %[ones], v[120:127], %[idx]\n\t" \
    "v_exp_f32_e32 v158, v158\n\t" \
    "v_exp_f32_e32 v159, v159\n\t" \
    "s_add_u32 %[cnt], %[cnt], 1\n\t" \
    "s_lshl_b32 %[tmp], %[cnt], 14\n\t" \
    "s_and_b32 %[tmp], %[tmp], 0x1ffff\n\t" \
    "v_add_u32 v192, %[tmp], %[lad]\n\t" \
    "s_bitcmp1_b32 %[bm], %[cnt]\n\t" \
    "s_cbranch_scc0 2f\n\t" \
    "s_waitcnt lgkmcnt(0)\n\t" \
    "s_barrier\n\t" \
    "2:\n\t" \
    "s_cmp_lt_u32 %[cnt], 16\n\t" \
    "s_cbranch_scc1 1b\n\t" \
    "s_waitcnt lgkmcnt(0)\n\t" \
    "s_nop 1\n\t" \
    "v_smfmac_f32_32x32x32_bf16 %[ob], v[184:187], v[144:151], %[idx]\n\t" \
    "v_smfmac_f32_16x16x64_bf16 %[lb], %[ones], v[144:151], %[idx]\n\t" \
    "v_smfmac_f32_32x32x32_bf16 %[ob], v[188:191], v[152:159], %[idx]\n\t" \
    "v_smfmac_f32_16x16x64_bf16 %[lb], %[ones], v[152:159], %[idx]\n\t" \
    "s_nop 7\n\t" \
    "s_nop 7\n\t" \
    "s_nop 7\n\t"
#define ATTN_CLOB "v96", "v97", "v98", "v99", "v100", "v101", "v102", "v103", "v104", "v105", "v106", "v107", "v108", "v109", "v110", "v111", "v112", "v113", "v114", "v115", "v116", "v117", "v118", "v119", "v120", "v121", "v122", "v123", "v124", "v125", "v126", "v127", "v128", "v129", "v130", "v131", "v132", "v133", "v134", "v135", "v136", "v137", "v138", "v139", "v140", "v141", "v142", "v143", "v144", "v145", "v146", "v147", "v148", "v149", "v150", "v151", "v152", "v153", "v154", "v155", "v156", "v157", "v158", "v159", "v160", "v161", "v162", "v163", "v164", "v165", "v166", "v167", "v168", "v169", "v170", "v171", "v172", "v173", "v174", "v175", "v176", "v177", "v178", "v179", "v180", "v181", "v182", "v183", "v184", "v185", "v186", "v187", "v188", "v189", "v190", "v191", "v192"

DI float rowmax16(const f32x16& s) {
  float m = fmaxf(fmaxf(s[0], s[1]), s[2]);
#pragma unroll
  for (int i = 3; i < 15; i += 2) m = fmaxf(fmaxf(m, s[i]), s[i + 1]);
  m = fmaxf(m, s[15]);
  auto rr = __builtin_amdgcn_permlane32_swap(__float_as_uint(m), __float_as_uint(m), false, false);
  return fmaxf(__uint_as_float(rr[0]), __uint_as_float(rr[1]));
}
DI void attn_finish(f32x16 o, const f32x4& l, const char* kvb, bf16x8 q0, bf16x8 q1, int lane, char* dst) {
  const float ls = (lane & 16) ? l[1] : l[0];
  o = o * __builtin_amdgcn_rcpf(ls);
  float chk = 0.f;
#pragma unroll
  for (int i = 0; i < 16; ++i) chk = __builtin_fmaf(o[i], 0.f, chk);
  const bool bad = !(ls < 1.2676506e30f) || !(chk == 0.f);
  if (__builtin_expect(__any(bad), 0)) attn_exact(kvb, q0, q1, lane, dst);
  else { *(bf16x8*)dst = pack8(o, 0); *(bf16x8*)(dst + 1024) = pack8(o, 8); }
}

__global__ __launch_bounds__(512, 2) void k2_attn(char* __restrict__ ws) {
  extern __shared__ __attribute__((aligned(16))) char smem[];
  lds_ptr lds = (lds_ptr)smem;
  const int lane = threadIdx.x & 63, wid = rfl(threadIdx.x >> 6), h = lane >> 5;
  const int head = blockIdx.x & 31, tt = (blockIdx.x >> 5) * 8 + (wid & 3) * 2;
  const unsigned lds0 = (unsigned)(uintptr_t)smem;
  const char* kvb = ws + WS_KVB;
  const size_t fo = ((size_t)tt * 32 + head) * 2048 + lane * 16;
  STAMP(1, 0);
  if (wid >= 4) {
    const int lw = wid - 4;
    const __amdgpu_buffer_rsrc_t skv = mk_srd(kvb, 262144);
    const unsigned vkv = lane * 16;
    auto issue = [&](int st) {
#pragma unroll
      for (int i = 0; i < 4; ++i) dma16(lds0 + (st & 7) * K2_SLOT + lw * 4096 + i * 1024, vkv, skv, st * 16384 + lw * 4096 + i * 1024);
    };
    if (lw < 2) dma16(lds0 + K2_K0 + lw * 1024, vkv, skv, 63 * 4096 + lw * 1024);
    issue(0);
    WAITV(0); BARRIER();
    issue(1); issue(2); issue(3);
    WAITV(0); BARRIER();
    issue(4); issue(5); issue(6); issue(7);
    {
      const f32x4* kp = (const f32x4*)(ws + WS_KVP + (size_t)head * 32768 + lw * 1024) + lane;
      const f32x4* zp = (const f32x4*)(ws + WS_ZP + (size_t)head * 1024 + h * 64 + lw * 16);
      f32x4 a = kp[0], b = zp[0];
#pragma unroll
      for (int i = 1; i < 8; ++i) { a += kp[i * 256]; b += zp[i * 8]; }
      *(__attribute__((address_space(3))) u32x2*)(lds + K2_EX + (lw >> 1) * 1024 + lane * 16 + (lw & 1) * 8) = u32x2{cvtpk(a[0], a[1]), cvtpk(a[2], a[3])};
      *(__attribute__((address_space(3))) u32x2*)(lds + K2_EX + 2048 + (lw >> 1) * 1024 + lane * 16 + (lw & 1) * 8) = u32x2{cvtpk(b[0], b[1]), cvtpk(b[2], b[3])};
    }
    u32x4 qa[4];
#pragma unroll
    for (int i = 0; i < 4; ++i) qa[i] = *(const u32x4*)(ws + WS_QA + fo + (i >> 1) * 65536 + (i & 1) * 1024);
    WAITV(0); BARRIER();
    issue(8); issue(9); issue(10); issue(11);
    {
      lds_ptr e = lds + K2_EX + lane * 16;
      const bf16x8 kv0 = lds128(e), kv1 = lds128(e + 1024), z0 = lds128(e + 2048), z1 = lds128(e + 3072);
#pragma unroll
      for (int j = 0; j < 2; ++j) {
        f32x16 pq;
#pragma unroll
        for (int i = 0; i < 4; ++i) {
          pq[2 * i] = phi(__uint_as_float(qa[2 * j][i] << 16)); pq[2 * i + 1] = phi(__uint_as_float(qa[2 * j][i] & 0xffff0000u));
          pq[8 + 2 * i] = phi(__uint_as_float(qa[2 * j + 1][i] << 16)); pq[8 + 2 * i + 1] = phi(__uint_as_float(qa[2 * j + 1][i] & 0xffff0000u));
        }
        const bf16x8 pq0 = pack8(pq, 0), pq1 = pack8(pq, 8);
        f32x16 num = MFMA32(kv0, pq0, f32x16{}); num = MFMA32(kv1, pq1, num);
        f32x16 den = MFMA32(z0, pq0, f32x16{}); den = MFMA32(z1, pq1, den);
        num = num * __builtin_amdgcn_rcpf(den[0] + 1e-6f);
        *(bf16x8*)(ws + WS_OA + fo + j * 65536) = pack8(num, 0); *(bf16x8*)(ws + WS_OA + fo + j * 65536 + 1024) = pack8(num, 8);
      }
    }
    WAITV(0); BARRIER();
    issue(12); issue(13); issue(14); issue(15);
    WAITV(0); BARRIER();
    STAMP(1, 3);
    return;
  }
  const bf16x8 qa0 = *(const bf16x8*)(ws + WS_QB + fo), qa1 = *(const bf16x8*)(ws + WS_QB + fo + 1024);
  const bf16x8 qb0 = *(const bf16x8*)(ws + WS_QB + fo + 65536), qb1 = *(const bf16x8*)(ws + WS_QB + fo + 65536 + 1024);
  __builtin_amdgcn_s_setprio(1);
  BARRIER();
  const bf16x8 k00 = lds128(lds + K2_K0 + lane * 16), k01 = lds128(lds + K2_K0 + 1024 + lane * 16);
  f32x16 na = MFMA32(k00, qa0, f32x16{}), nb = MFMA32(k00, qb0, f32x16{});
  na = MFMA32(k01, qa1, na); nb = MFMA32(k01, qb1, nb);
  { const float ma = -rowmax16(na), mb = -rowmax16(nb);
#pragma unroll
    for (int i = 0; i < 16; ++i) { na[i] = ma; nb[i] = mb; } }
  const unsigned one0 = (lane & 1) ? 0u : 0x3f803f80u, one1 = one0 ^ 0x3f803f80u;
  const u32x4 ones = {one0, one0, one1, one1};
  f32x16 oa = {}, ob = {}; f32x4 la = {}, lb = {};
  STAMP(1, 1);
  {
    unsigned cnt, tmp;
    asm volatile("s_nop 1\n\t" ATTN_ASM
                 : [oa] "+v"(oa), [ob] "+v"(ob), [la] "+v"(la), [lb] "+v"(lb), [cnt] "=&s"(cnt), [tmp] "=&s"(tmp)
                 : [qa0] "v"(qa0), [qa1] "v"(qa1), [qb0] "v"(qb0), [qb1] "v"(qb1), [na] "v"(na), [nb] "v"(nb), [ones] "v"(ones), [idx] "v"(0xddddddddu),
                   [ka] "v"(lds0 + K2_K0 + lane * 16), [lad] "v"(lds0 + lane * 16), [bm] "s"(0x1112)
                 : "memory", "scc", ATTN_CLOB);
  }
  STAMP(1, 2);
  attn_finish(oa, la, kvb, qa0, qa1, lane, ws + WS_OB + fo);
  attn_finish(ob, lb, kvb, qb0, qb1, lane, ws + WS_OB + fo + 65536);
  STAMP(1, 3);
}

constexpr int K3_SLOT = 32768, K3_NS = 4, K3_LDS = K3_NS * K3_SLOT;

__global__ __launch_bounds__(512, 2) void k3_out(const char* __restrict__ ws, const char* __restrict__ woA, const char* __restrict__ woB, char* __restrict__ out) {
  extern __shared__ __attribute__((aligned(16))) char smem[];
  lds_ptr lds = (lds_ptr)smem;
  const int lane = threadIdx.x & 63, wid = rfl(threadIdx.x >> 6);
  const int xcd = blockIdx.x & 7, idx = blockIdx.x >> 3, lr = idx & 3, lc = idx >> 2;
  const int br = xcd >> 2, rt = (xcd & 3) * 4 + lr;
  STAMP(2, 0);
  if (wid >= 4) {
    const int lw = wid - 4;
    const unsigned lds0 = (unsigned)(uintptr_t)smem;
    const __amdgpu_buffer_rsrc_t sa = mk_srd(ws + (br ? WS_OB : WS_OA), 4u << 20), sw = mk_srd(br ? woB : woA, 2u << 20);
    const unsigned va = lane * 16, vw = (lane >> 3) * 2048 + (((lane & 7) ^ (((lane >> 4) & 1) << 2)) << 4);
    const unsigned sa0 = (rt * 4 + lw) * 65536, sw0 = (lw & 1) * 32 * 2048 + lc * 256 + (lw >> 1) * 128;
    const unsigned da = lds0 + lw * 4096, dw = lds0 + 16384 + lw * 4096;
    const int k0 = K0B;
    auto issue = [&](int st) {
      const unsigned kt = (st + k0) & 15, slot = (st & 3) * K3_SLOT;
#pragma unroll
      for (int i = 0; i < 4; ++i) dma16(da + slot + i * 1024, va, sa, sa0 + kt * 4096 + i * 1024);
#pragma unroll
      for (int i = 0; i < 4; ++i) dma16(dw + slot + i * 1024, vw, sw, sw0 + (kt * 64 + i * 8) * 2048);
    };
    issue(0); issue(1); issue(2); issue(3);
    WAITV(24);
    BARRIER();
    for (int st = 1; st < 13; ++st) { WAITV(16); BARRIER(); issue(st + 3); }
    WAITV(16); BARRIER();
    WAITV(8); BARRIER();
    WAITV(0); BARRIER();
    BARRIER();
    return;
  }
  const int wr = wid >> 1, wc = wid & 1;
  const int h = lane >> 5, q = (lane & 15) >> 2, p = lane & 3, blk = (lane >> 4) & 1;
  f32x16 acc[2][2];
  {
    const unsigned lds0 = (unsigned)(uintptr_t)smem;
    unsigned slot, cnt;
#define ACC4_OUT "=&{v[0:15]}"(acc[0][0]), "=&{v[16:31]}"(acc[0][1]), "=&{v[32:47]}"(acc[1][0]), "=&{v[48:63]}"(acc[1][1])
    const unsigned wa0 = lds0 + 16384 + wc * 8192 + (4 * h + q) * 128 + ((q >> 1) << 6) + blk * 32 + p * 8;
    asm volatile(G3_ASM : ACC4_OUT, [slot] "=&s"(slot), [cnt] "=&s"(cnt) : [xa0] "v"(lds0 + wr * 8192 + lane * 16), [wa0] "v"(wa0), [wa1] "v"(wa0 ^ 64) : "memory", "scc", G3_CLOB);
  }
  STAMP(2, 2);
  char* o = out + (size_t)(rt * 128 + wr * 64 + (lane & 31)) * 4096 + br * 2048 + lc * 256 + wc * 128 + h * 16;
#pragma unroll
  for (int mt = 0; mt < 2; ++mt)
#pragma unroll
    for (int nt = 0; nt < 2; ++nt)
#pragma unroll
      for (int g = 0; g < 2; ++g) {
        const f32x16& v = acc[mt][nt];
        unsigned x0 = cvtpk(v[8 * g], v[8 * g + 1]), x1 = cvtpk(v[8 * g + 2], v[8 * g + 3]), y0 = cvtpk(v[8 * g + 4], v[8 * g + 5]), y1 = cvtpk(v[8 * g + 6], v[8 * g + 7]);
        auto r0 = __builtin_amdgcn_permlane32_swap(x0, y0, false, false); auto r1 = __builtin_amdgcn_permlane32_swap(x1, y1, false, false);
        *(u32x4*)(o + mt * 32 * 4096 + nt * 64 + g * 32) = u32x4{r0[0], r1[0], r0[1], r1[1]};
      }
  STAMP(2, 3);
}

extern "C" void kernel_launch(void* const* d_in, const int* in_sizes, int n_in, void* d_out, int out_size, void* d_ws, size_t ws_size, hipStream_t stream) {
  (void)hipFuncSetAttribute((const void*)k1_proj, hipFuncAttributeMaxDynamicSharedMemorySize, K1_LDS);
  (void)hipFuncSetAttribute((const void*)k2_attn, hipFuncAttributeMaxDynamicSharedMemorySize, K2_LDS);
  (void)hipFuncSetAttribute((const void*)k3_out, hipFuncAttributeMaxDynamicSharedMemorySize, K3_LDS);
  char* ws = (char*)d_ws;
  k1_proj<<<256, 512, K1_LDS, stream>>>((const char*)d_in[0], (const char*)d_in[1], (const char*)d_in[3], (const char*)d_in[4], (const char*)d_in[5], (const char*)d_in[6], ws);
  k2_attn<<<256, 512, K2_LDS, stream>>>(ws);
  k3_out<<<256, 512, K3_LDS, stream>>>(ws, (const char*)d_in[2], (const char*)d_in[7], (char*)d_out);
}
```

```cpp
#include <hip/hip_runtime.h>
#include <hip/hip_bf16.h>
#include <stdint.h>

typedef __bf16 bf16x8 __attribute__((ext_vector_type(8)));
typedef __bf16 bf16x2 __attribute__((ext_vector_type(2)));
typedef float f32x16 __attribute__((ext_vector_type(16)));
typedef float f32x8 __attribute__((ext_vector_type(8)));
typedef float f32x4 __attribute__((ext_vector_type(4)));
typedef float f32x2 __attribute__((ext_vector_type(2)));
typedef unsigned u32x4 __attribute__((ext_vector_type(4)));
typedef unsigned u32x2 __attribute__((ext_vector_type(2)));
typedef unsigned long long u64;
typedef __attribute__((address_space(3))) char* lds_ptr;
typedef __attribute__((address_space(1))) u64 gu64;
typedef __amdgpu_buffer_rsrc_t srd_t;
#define LAS __attribute__((address_space(3)))
#define DI __device__ __forceinline__
#define MFMA32(a, b, c) __builtin_amdgcn_mfma_f32_32x32x16_bf16(a, b, c, 0, 0, 0)
#define MFMA16(a, b, c) __builtin_amdgcn_mfma_f32_16x16x32_bf16(a, b, c, 0, 0, 0)
#define BC8(x) __builtin_bit_cast(bf16x8, x)

constexpr unsigned WS_E = 0, WS_F1 = 4096, WS_F2 = 65536, WS_F3 = 196608, WS_F4 = 229376, WS_F5 = 262144;
constexpr unsigned WS_KVF = 3u << 18, WS_KVB = 1u << 20, WS_ZP = 3u << 19, WS_KVP = 2u << 20, WS_OA = 4u << 20, WS_OB = 8u << 20, WS_BYTES = 12u << 20;
constexpr unsigned P1_SLOT = 40960, P1_W = 16384, L_PQ = 131072, L_EXZ = 147456, L_XZ = L_EXZ + 128, L_K0 = L_XZ + 512, L_XCH = 114688, LDS_BYTES = L_K0 + 2048;
constexpr float QSCALE = 0.17677669529663687f * 1.4426950408889634f;
constexpr float LOG2E = 1.4426950408889634f;

DI unsigned rfl(unsigned x) { return __builtin_amdgcn_readfirstlane(x); }
DI unsigned cvtpk(float lo, float hi) { f32x2 v = {lo, hi}; return __builtin_bit_cast(unsigned, __builtin_convertvector(v, bf16x2)); }
DI u32x4 pack8(const f32x16& v, int o) { return u32x4{cvtpk(v[o], v[o + 1]), cvtpk(v[o + 2], v[o + 3]), cvtpk(v[o + 4], v[o + 5]), cvtpk(v[o + 6], v[o + 7])}; }
DI u32x4 pack44(const f32x16& a, int oa, const f32x16& b, int ob) { return u32x4{cvtpk(a[oa], a[oa + 1]), cvtpk(a[oa + 2], a[oa + 3]), cvtpk(b[ob], b[ob + 1]), cvtpk(b[ob + 2], b[ob + 3])}; }
DI srd_t mk_srd(const void* p, unsigned bytes) { return __builtin_amdgcn_make_buffer_rsrc((void*)p, 0, bytes, 0x00020000); }
DI void dma16(unsigned ldsaddr, unsigned voff, srd_t srd, unsigned soff) {
  asm volatile("s_mov_b32 m0, %0\n\ts_nop 0\n\tbuffer_load_dwordx4 %1, %2, %3 offen lds" ::"s"(ldsaddr), "v"(voff), "s"(srd), "s"(soff) : "memory");
}
DI void dma16c(unsigned ldsaddr, unsigned voff, srd_t srd, unsigned soff) {
  asm volatile("s_mov_b32 m0, %0\n\ts_nop 0\n\tbuffer_load_dwordx4 %1, %2, %3 offen sc1 lds" ::"s"(ldsaddr), "v"(voff), "s"(srd), "s"(soff) : "memory");
}
#define WAITV(n) asm volatile("s_waitcnt vmcnt(%0)" ::"n"(n) : "memory")
#define VBAR(n) asm volatile("s_waitcnt vmcnt(%0)\n\ts_barrier" ::"n"(n) : "memory")
#define BARRIER() asm volatile("s_waitcnt lgkmcnt(0)\n\ts_barrier" ::: "memory")
DI bf16x8 lds128(lds_ptr p) { return *(const LAS bf16x8*)p; }
DI void st16(srd_t s, unsigned off, u32x4 v) { __builtin_amdgcn_raw_buffer_store_b128(v, s, off, 0, 16); }
DI void st8(srd_t s, unsigned off, u32x2 v) { __builtin_amdgcn_raw_buffer_store_b64(v, s, off, 0, 16); }
DI u32x4 ld16(srd_t s, unsigned off) { return __builtin_amdgcn_raw_buffer_load_b128(s, off, 0, 16); }
DI void set_flag(char* ws, unsigned off, u64 tag) { __hip_atomic_store((gu64*)(ws + off), tag, __ATOMIC_RELAXED, __HIP_MEMORY_SCOPE_AGENT); }
DI bool wait_flags(char* ws, unsigned off, u64 tag, unsigned cap) {
  bool ok = false;
#pragma unroll 1
  for (unsigned spins = 0; spins < cap; ++spins) {
    ok = __all(__hip_atomic_load((gu64*)(ws + off), __ATOMIC_RELAXED, __HIP_MEMORY_SCOPE_AGENT) == tag);
    if (ok) break;
    __builtin_amdgcn_s_sleep(2);
  }
  asm volatile("" ::: "memory");
  return ok;
}
constexpr unsigned SPIN_CAP = 1u << 20;
DI float adds(float a, float b) { asm("v_add_f32_e32 %0, %0, %1" : "+v"(a) : "v"(b)); return a; }
DI float muls(float a, float b) { asm("v_mul_f32_e32 %0, %0, %1" : "+v"(a) : "v"(b)); return a; }
DI f32x4 add4(f32x4 a, f32x4 b) { return f32x4{adds(a[0], b[0]), adds(a[1], b[1]), adds(a[2], b[2]), adds(a[3], b[3])}; }
DI float maxs(float a, float b) { asm("v_max_f32_e32 %0, %1, %0" : "+v"(b) : "s"(a)); return b; }
DI void phi4(f32x16& v, int o) {
#pragma unroll
  for (int i = 0; i < 4; i += 2) {
    const f32x2 t = {v[o + i], v[o + i + 1]}, u = t * LOG2E;
    const f32x2 e = {__builtin_amdgcn_fmed3f(__builtin_amdgcn_exp2f(u[0]), 0.f, 1.f), __builtin_amdgcn_fmed3f(__builtin_amdgcn_exp2f(u[1]), 0.f, 1.f)};
    const f32x2 r = f32x2{maxs(1e-6f, t[0]), maxs(1e-6f, t[1])} + e;
    v[o + i] = r[0]; v[o + i + 1] = r[1];
  }
}
DI float xsum2(float v) {
  auto a = __builtin_amdgcn_permlane16_swap(__float_as_uint(v), __float_as_uint(v), false, false); v = __uint_as_float(a[0]) + __uint_as_float(a[1]);
  auto b = __builtin_amdgcn_permlane32_swap(__float_as_uint(v), __float_as_uint(v), false, false); return __uint_as_float(b[0]) + __uint_as_float(b[1]);
}
DI float xsum(float v) {
  return xsum2(v + __builtin_bit_cast(float, __builtin_amdgcn_update_dpp(0, __builtin_bit_cast(int, v), 0x128, 0xf, 0xf, true)));
}
#define GB0_ASM \
    "s_mov_b32 %[cnt], 16\n\t" \
    "v_add_u32 v80, %[slot], %[xa0]\n\t" \
    "v_add_u32 v81, %[slot], %[xa1]\n\t" \
    "v_add_u32 v82, %[slot], %[wa0]\n\t" \
    "v_add_u32 v83, %[slot], %[wa1]\n\t" \
    "s_barrier\n\t" \
    "ds_read_b128 v[32:35], v80 offset:0\n\t" \
    "ds_read_b64_tr_b16 v[48:49], v82 offset:0\n\t" \
    "ds_read_b64_tr_b16 v[50:51], v82 offset:512\n\t" \
    "ds_read_b64_tr_b16 v[52:53], v83 offset:0\n\t" \
    "ds_read_b64_tr_b16 v[54:55], v83 offset:512\n\t" \
    "ds_read_b128 v[36:39], v80 offset:2048\n\t" \
    "ds_read_b128 v[40:43], v80 offset:4096\n\t" \
    "ds_read_b128 v[44:47], v80 offset:6144\n\t" \
    "s_waitcnt lgkmcnt(0)\n\t" \
    "v_mfma_f32_16x16x32_bf16 v[0:3], v[48:51], v[32:35], 0\n\t" \
    "ds_read_b128 v[56:59], v81 offset:0\n\t" \
    "ds_read_b64_tr_b16 v[72:73], v82 offset:4096\n\t" \
    "v_mfma_f32_16x16x32_bf16 v[4:7], v[52:55], v[32:35], 0\n\t" \
    "ds_read_b64_tr_b16 v[74:75], v82 offset:4608\n\t" \
    "ds_read_b64_tr_b16 v[76:77], v83 offset:4096\n\t" \
    "v_mfma_f32_16x16x32_bf16 v[8:11], v[48:51], v[36:39], 0\n\t" \
    "ds_read_b64_tr_b16 v[78:79], v83 offset:4608\n\t" \
    "ds_read_b128 v[60:63], v81 offset:2048\n\t" \
    "v_mfma_f32_16x16x32_bf16 v[12:15], v[52:55], v[36:39], 0\n\t" \
    "ds_read_b128 v[64:67], v81 offset:4096\n\t" \
    "ds_read_b128 v[68:71], v81 offset:6144\n\t" \
    "v_mfma_f32_16x16x32_bf16 v[16:19], v[48:51], v[40:43], 0\n\t" \
    "s_add_u32 %[slot], %[slot], 40960\n\t" \
    "s_cmp_eq_u32 %[slot], 122880\n\t" \
    "s_cselect_b32 %[slot], 0, %[slot]\n\t" \
    "v_add_u32 v80, %[slot], %[xa0]\n\t" \
    "v_mfma_f32_16x16x32_bf16 v[20:23], v[52:55], v[40:43], 0\n\t" \
    "v_add_u32 v81, %[slot], %[xa1]\n\t" \
    "v_add_u32 v82, %[slot], %[wa0]\n\t" \
    "v_mfma_f32_16x16x32_bf16 v[24:27], v[48:51], v[44:47], 0\n\t" \
    "v_add_u32 v83, %[slot], %[wa1]\n\t" \
    "v_mfma_f32_16x16x32_bf16 v[28:31], v[52:55], v[44:47], 0\n\t" \
    "s_branch 6f\n\t" \
    ".p2align 6\n\t" \
    "1:\n\t" \
    "s_waitcnt lgkmcnt(0)\n\t" \
    "v_mfma_f32_16x16x32_bf16 v[0:3], v[48:51], v[32:35], v[0:3]\n\t" \
    "ds_read_b128 v[56:59], v81 offset:0\n\t" \
    "ds_read_b64_tr_b16 v[72:73], v82 offset:4096\n\t" \
    "v_mfma_f32_16x16x32_bf16 v[4:7], v[52:55], v[32:35], v[4:7]\n\t" \
    "ds_read_b64_tr_b16 v[74:75], v82 offset:4608\n\t" \
    "ds_read_b64_tr_b16 v[76:77], v83 offset:4096\n\t" \
    "v_mfma_f32_16x16x32_bf16 v[8:11], v[48:51], v[36:39], v[8:11]\n\t" \
    "ds_read_b64_tr_b16 v[78:79], v83 offset:4608\n\t" \
    "ds_read_b128 v[60:63], v81 offset:2048\n\t" \
    "v_mfma_f32_16x16x32_bf16 v[12:15], v[52:55], v[36:39], v[12:15]\n\t" \
    "ds_read_b128 v[64:67], v81 offset:4096\n\t" \
    "ds_read_b128 v[68:71], v81 offset:6144\n\t" \
    "v_mfma_f32_16x16x32_bf16 v[16:19], v[48:51], v[40:43], v[16:19]\n\t" \
    "s_add_u32 %[slot], %[slot], 40960\n\t" \
    "s_cmp_eq_u32 %[slot], 122880\n\t" \
    "s_cselect_b32 %[slot], 0, %[slot]\n\t" \
    "v_add_u32 v80, %[slot], %[xa0]\n\t" \
    "v_mfma_f32_16x16x32_bf16 v[20:23], v[52:55], v[40:43], v[20:23]\n\t" \
    "v_add_u32 v81, %[slot], %[xa1]\n\t" \
    "v_add_u32 v82, %[slot], %[wa0]\n\t" \
    "v_mfma_f32_16x16x32_bf16 v[24:27], v[48:51], v[44:47], v[24:27]\n\t" \
    "v_add_u32 v83, %[slot], %[wa1]\n\t" \
    "v_mfma_f32_16x16x32_bf16 v[28:31], v[52:55], v[44:47], v[28:31]\n\t" \
    "6:\n\t" \
    "s_waitcnt lgkmcnt(0)\n\t" \
    "v_mfma_f32_16x16x32_bf16 v[0:3], v[72:75], v[56:59], v[0:3]\n\t" \
    "v_mfma_f32_16x16x32_bf16 v[4:7], v[76:79], v[56:59], v[4:7]\n\t" \
    "s_barrier\n\t" \
    "ds_read_b128 v[32:35], v80 offset:0\n\t" \
    "ds_read_b64_tr_b16 v[48:49], v82 offset:0\n\t" \
    "v_mfma_f32_16x16x32_bf16 v[8:11], v[72:75], v[60:63], v[8:11]\n\t" \
    "ds_read_b64_tr_b16 v[50:51], v82 offset:512\n\t" \
    "ds_read_b64_tr_b16 v[52:53], v83 offset:0\n\t" \
    "v_mfma_f32_16x16x32_bf16 v[12:15], v[76:79], v[60:63], v[12:15]\n\t" \
    "ds_read_b64_tr_b16 v[54:55], v83 offset:512\n\t" \
    "ds_read_b128 v[36:39], v80 offset:2048\n\t" \
    "v_mfma_f32_16x16x32_bf16 v[16:19], v[72:75], v[64:67], v[16:19]\n\t" \
    "ds_read_b128 v[40:43], v80 offset:4096\n\t" \
    "ds_read_b128 v[44:47], v80 offset:6144\n\t" \
    "v_mfma_f32_16x16x32_bf16 v[20:23], v[76:79], v[64:67], v[20:23]\n\t" \
    "s_sub_u32 %[cnt], %[cnt], 1\n\t" \
    "s_cmp_lg_u32 %[cnt], 0\n\t" \
    "v_mfma_f32_16x16x32_bf16 v[24:27], v[72:75], v[68:71], v[24:27]\n\t" \
    "v_mfma_f32_16x16x32_bf16 v[28:31], v[76:79], v[68:71], v[28:31]\n\t" \
    "s_cbranch_scc1 1b\n\t" \
    "s_waitcnt lgkmcnt(0)\n\t" \
    "s_nop 7\n\t"
#define GB0_CLOB "v32", "v33", "v34", "v35", "v36", "v37", "v38", "v39", "v40", "v41", "v42", "v43", "v44", "v45", "v46", "v47", "v48", "v49", "v50", "v51", "v52", "v53", "v54", "v55", "v56", "v57", "v58", "v59", "v60", "v61", "v62", "v63", "v64", "v65", "v66", "v67", "v68", "v69", "v70", "v71", "v72", "v73", "v74", "v75", "v76", "v77", "v78", "v79", "v80", "v81", "v82", "v83"
#define GBK_ASM \
    "s_mov_b32 %[cnt], 16\n\t" \
    "v_add_u32 v112, %[slot], %[xa0]\n\t" \
    "v_add_u32 v113, %[slot], %[xa1]\n\t" \
    "v_add_u32 v114, %[slot], %[wa0]\n\t" \
    "v_add_u32 v115, %[slot], %[wa1]\n\t" \
    "v_add_u32 v116, %[xe], v112\n\t" \
    "v_add_u32 v117, %[xe], v113\n\t" \
    "v_add_u32 v118, %[slot], %[ea0]\n\t" \
    "v_add_u32 v119, %[slot], %[ea1]\n\t" \
    "s_barrier\n\t" \
    "ds_read_b128 v[40:43], v112 offset:0\n\t" \
    "ds_read_b64_tr_b16 v[56:57], v114 offset:0\n\t" \
    "ds_read_b64_tr_b16 v[58:59], v114 offset:512\n\t" \
    "ds_read_b64_tr_b16 v[60:61], v115 offset:0\n\t" \
    "ds_read_b64_tr_b16 v[62:63], v115 offset:512\n\t" \
    "ds_read_b128 v[44:47], v112 offset:2048\n\t" \
    "ds_read_b128 v[48:51], v112 offset:4096\n\t" \
    "ds_read_b128 v[52:55], v112 offset:6144\n\t" \
    "ds_read_b128 v[64:67], v116\n\t" \
    "ds_read_b64_tr_b16 v[68:69], v118 offset:0\n\t" \
    "ds_read_b64_tr_b16 v[70:71], v118 offset:256\n\t" \
    "ds_read_b64_tr_b16 v[72:73], v119 offset:0\n\t" \
    "ds_read_b64_tr_b16 v[74:75], v119 offset:256\n\t" \
    "s_waitcnt lgkmcnt(0)\n\t" \
    "v_mfma_f32_16x16x32_bf16 v[0:3], v[56:59], v[40:43], 0\n\t" \
    "ds_read_b128 v[76:79], v113 offset:0\n\t" \
    "ds_read_b64_tr_b16 v[92:93], v114 offset:4096\n\t" \
    "v_mfma_f32_16x16x32_bf16 v[4:7], v[60:63], v[40:43], 0\n\t" \
    "ds_read_b64_tr_b16 v[94:95], v114 offset:4608\n\t" \
    "ds_read_b64_tr_b16 v[96:97], v115 offset:4096\n\t" \
    "v_mfma_f32_16x16x32_bf16 v[8:11], v[56:59], v[44:47], 0\n\t" \
    "ds_read_b64_tr_b16 v[98:99], v115 offset:4608\n\t" \
    "ds_read_b128 v[80:83], v113 offset:2048\n\t" \
    "v_mfma_f32_16x16x32_bf16 v[12:15], v[60:63], v[44:47], 0\n\t" \
    "ds_read_b128 v[84:87], v113 offset:4096\n\t" \
    "ds_read_b128 v[88:91], v113 offset:6144\n\t" \
    "v_mfma_f32_16x16x32_bf16 v[16:19], v[56:59], v[48:51], 0\n\t" \
    "ds_read_b128 v[100:103], v117\n\t" \
    "ds_read_b64_tr_b16 v[104:105], v118 offset:2048\n\t" \
    "v_mfma_f32_16x16x32_bf16 v[20:23], v[60:63], v[48:51], 0\n\t" \
    "ds_read_b64_tr_b16 v[106:107], v118 offset:2304\n\t" \
    "ds_read_b64_tr_b16 v[108:109], v119 offset:2048\n\t" \
    "v_mfma_f32_16x16x32_bf16 v[24:27], v[56:59], v[52:55], 0\n\t" \
    "ds_read_b64_tr_b16 v[110:111], v119 offset:2304\n\t" \
    "s_add_u32 %[slot], %[slot], 40960\n\t" \
    "s_cmp_eq_u32 %[slot], 122880\n\t" \
    "s_cselect_b32 %[slot], 0, %[slot]\n\t" \
    "v_mfma_f32_16x16x32_bf16 v[28:31], v[60:63], v[52:55], 0\n\t" \
    "v_add_u32 v112, %[slot], %[xa0]\n\t" \
    "v_add_u32 v113, %[slot], %[xa1]\n\t" \
    "v_mfma_f32_16x16x32_bf16 v[32:35], v[68:71], v[64:67], 0\n\t" \
    "v_add_u32 v114, %[slot], %[wa0]\n\t" \
    "v_add_u32 v115, %[slot], %[wa1]\n\t" \
    "v_mfma_f32_16x16x32_bf16 v[36:39], v[72:75], v[64:67], 0\n\t" \
    "v_add_u32 v116, %[xe], v112\n\t" \
    "v_add_u32 v117, %[xe], v113\n\t" \
    "v_add_u32 v118, %[slot], %[ea0]\n\t" \
    "v_add_u32 v119, %[slot], %[ea1]\n\t" \
    "s_branch 6f\n\t" \
    ".p2align 6\n\t" \
    "1:\n\t" \
    "s_waitcnt lgkmcnt(0)\n\t" \
    "v_mfma_f32_16x16x32_bf16 v[0:3], v[56:59], v[40:43], v[0:3]\n\t" \
    "ds_read_b128 v[76:79], v113 offset:0\n\t" \
    "ds_read_b64_tr_b16 v[92:93], v114 offset:4096\n\t" \
    "v_mfma_f32_16x16x32_bf16 v[4:7], v[60:63], v[40:43], v[4:7]\n\t" \
    "ds_read_b64_tr_b16 v[94:95], v114 offset:4608\n\t" \
    "ds_read_b64_tr_b16 v[96:97], v115 offset:4096\n\t" \
    "v_mfma_f32_16x16x32_bf16 v[8:11], v[56:59], v[44:47], v[8:11]\n\t" \
    "ds_read_b64_tr_b16 v[98:99], v115 offset:4608\n\t" \
    "ds_read_b128 v[80:83], v113 offset:2048\n\t" \
    "v_mfma_f32_16x16x32_bf16 v[12:15], v[60:63], v[44:47], v[12:15]\n\t" \
    "ds_read_b128 v[84:87], v113 offset:4096\n\t" \
    "ds_read_b128 v[88:91], v113 offset:6144\n\t" \
    "v_mfma_f32_16x16x32_bf16 v[16:19], v[56:59], v[48:51], v[16:19]\n\t" \
    "ds_read_b128 v[100:103], v117\n\t" \
    "ds_read_b64_tr_b16 v[104:105], v118 offset:2048\n\t" \
    "v_mfma_f32_16x16x32_bf16 v[20:23], v[60:63], v[48:51], v[20:23]\n\t" \
    "ds_read_b64_tr_b16 v[106:107], v118 offset:2304\n\t" \
    "ds_read_b64_tr_b16 v[108:109], v119 offset:2048\n\t" \
    "v_mfma_f32_16x16x32_bf16 v[24:27], v[56:59], v[52:55], v[24:27]\n\t" \
    "ds_read_b64_tr_b16 v[110:111], v119 offset:2304\n\t" \
    "s_add_u32 %[slot], %[slot], 40960\n\t" \
    "s_cmp_eq_u32 %[slot], 122880\n\t" \
    "s_cselect_b32 %[slot], 0, %[slot]\n\t" \
    "v_mfma_f32_16x16x32_bf16 v[28:31], v[60:63], v[52:55], v[28:31]\n\t" \
    "v_add_u32 v112, %[slot], %[xa0]\n\t" \
    "v_add_u32 v113, %[slot], %[xa1]\n\t" \
    "v_mfma_f32_16x16x32_bf16 v[32:35], v[68:71], v[64:67], v[32:35]\n\t" \
    "v_add_u32 v114, %[slot], %[wa0]\n\t" \
    "v_add_u32 v115, %[slot], %[wa1]\n\t" \
    "v_mfma_f32_16x16x32_bf16 v[36:39], v[72:75], v[64:67], v[36:39]\n\t" \
    "v_add_u32 v116, %[xe], v112\n\t" \
    "v_add_u32 v117, %[xe], v113\n\t" \
    "v_add_u32 v118, %[slot], %[ea0]\n\t" \
    "v_add_u32 v119, %[slot], %[ea1]\n\t" \
    "6:\n\t" \
    "s_waitcnt lgkmcnt(0)\n\t" \
    "v_mfma_f32_16x16x32_bf16 v[0:3], v[92:95], v[76:79], v[0:3]\n\t" \
    "v_mfma_f32_16x16x32_bf16 v[4:7], v[96:99], v[76:79], v[4:7]\n\t" \
    "s_barrier\n\t" \
    "ds_read_b128 v[40:43], v112 offset:0\n\t" \
    "ds_read_b64_tr_b16 v[56:57], v114 offset:0\n\t" \
    "v_mfma_f32_16x16x32_bf16 v[8:11], v[92:95], v[80:83], v[8:11]\n\t" \
    "ds_read_b64_tr_b16 v[58:59], v114 offset:512\n\t" \
    "ds_read_b64_tr_b16 v[60:61], v115 offset:0\n\t" \
    "v_mfma_f32_16x16x32_bf16 v[12:15], v[96:99], v[80:83], v[12:15]\n\t" \
    "ds_read_b64_tr_b16 v[62:63], v115 offset:512\n\t" \
    "ds_read_b128 v[44:47], v112 offset:2048\n\t" \
    "v_mfma_f32_16x16x32_bf16 v[16:19], v[92:95], v[84:87], v[16:19]\n\t" \
    "ds_read_b128 v[48:51], v112 offset:4096\n\t" \
    "ds_read_b128 v[52:55], v112 offset:6144\n\t" \
    "v_mfma_f32_16x16x32_bf16 v[20:23], v[96:99], v[84:87], v[20:23]\n\t" \
    "ds_read_b128 v[64:67], v116\n\t" \
    "ds_read_b64_tr_b16 v[68:69], v118 offset:0\n\t" \
    "v_mfma_f32_16x16x32_bf16 v[24:27], v[92:95], v[88:91], v[24:27]\n\t" \
    "ds_read_b64_tr_b16 v[70:71], v118 offset:256\n\t" \
    "ds_read_b64_tr_b16 v[72:73], v119 offset:0\n\t" \
    "v_mfma_f32_16x16x32_bf16 v[28:31], v[96:99], v[88:91], v[28:31]\n\t" \
    "ds_read_b64_tr_b16 v[74:75], v119 offset:256\n\t" \
    "s_sub_u32 %[cnt], %[cnt], 1\n\t" \
    "s_cmp_lg_u32 %[cnt], 0\n\t" \
    "v_mfma_f32_16x16x32_bf16 v[32:35], v[104:107], v[100:103], v[32:35]\n\t" \
    "v_mfma_f32_16x16x32_bf16 v[36:39], v[108:111], v[100:103], v[36:39]\n\t" \
    "s_cbranch_scc1 1b\n\t" \
    "s_waitcnt lgkmcnt(0)\n\t" \
    "s_nop 7\n\t"
#define GBK_CLOB "v40", "v41", "v42", "v43", "v44", "v45", "v46", "v47", "v48", "v49", "v50", "v51", "v52", "v53", "v54", "v55", "v56", "v57", "v58", "v59", "v60", "v61", "v62", "v63", "v64", "v65", "v66", "v67", "v68", "v69", "v70", "v71", "v72", "v73", "v74", "v75", "v76", "v77", "v78", "v79", "v80", "v81", "v82", "v83", "v84", "v85", "v86", "v87", "v88", "v89", "v90", "v91", "v92", "v93", "v94", "v95", "v96", "v97", "v98", "v99", "v100", "v101", "v102", "v103", "v104", "v105", "v106", "v107", "v108", "v109", "v110", "v111", "v112", "v113", "v114", "v115", "v116", "v117", "v118", "v119"
#define GBV_ASM \
    "s_mov_b32 %[cnt], 16\n\t" \
    "v_add_u32 v112, %[slot], %[xa0]\n\t" \
    "v_add_u32 v113, %[slot], %[xa1]\n\t" \
    "v_add_u32 v114, %[slot], %[wa0]\n\t" \
    "v_add_u32 v115, %[slot], %[wa1]\n\t" \
    "v_add_u32 v116, %[xe], v112\n\t" \
    "v_add_u32 v117, %[xe], v113\n\t" \
    "v_add_u32 v118, %[slot], %[ea0]\n\t" \
    "v_add_u32 v119, %[slot], %[ea1]\n\t" \
    "s_barrier\n\t" \
    "ds_read_b128 v[40:43], v112 offset:0\n\t" \
    "ds_read_b64_tr_b16 v[56:57], v114 offset:0\n\t" \
    "ds_read_b64_tr_b16 v[58:59], v114 offset:512\n\t" \
    "ds_read_b64_tr_b16 v[60:61], v115 offset:0\n\t" \
    "ds_read_b64_tr_b16 v[62:63], v115 offset:512\n\t" \
    "ds_read_b128 v[44:47], v112 offset:2048\n\t" \
    "ds_read_b128 v[48:51], v112 offset:4096\n\t" \
    "ds_read_b128 v[52:55], v112 offset:6144\n\t" \
    "ds_read_b128 v[64:67], v116\n\t" \
    "ds_read_b64_tr_b16 v[68:69], v118 offset:0\n\t" \
    "ds_read_b64_tr_b16 v[70:71], v118 offset:256\n\t" \
    "ds_read_b64_tr_b16 v[72:73], v119 offset:0\n\t" \
    "ds_read_b64_tr_b16 v[74:75], v119 offset:256\n\t" \
    "s_waitcnt lgkmcnt(0)\n\t" \
    "v_mfma_f32_16x16x32_bf16 v[0:3], v[56:59], v[40:43], 0\n\t" \
    "ds_read_b128 v[76:79], v113 offset:0\n\t" \
    "ds_read_b64_tr_b16 v[92:93], v114 offset:4096\n\t" \
    "v_mfma_f32_16x16x32_bf16 v[4:7], v[60:63], v[40:43], 0\n\t" \
    "ds_read_b64_tr_b16 v[94:95], v114 offset:4608\n\t" \
    "ds_read_b64_tr_b16 v[96:97], v115 offset:4096\n\t" \
    "v_mfma_f32_16x16x32_bf16 v[8:11], v[56:59], v[44:47], 0\n\t" \
    "ds_read_b64_tr_b16 v[98:99], v115 offset:4608\n\t" \
    "ds_read_b128 v[80:83], v113 offset:2048\n\t" \
    "v_mfma_f32_16x16x32_bf16 v[12:15], v[60:63], v[44:47], 0\n\t" \
    "ds_read_b128 v[84:87], v113 offset:4096\n\t" \
    "ds_read_b128 v[88:91], v113 offset:6144\n\t" \
    "v_mfma_f32_16x16x32_bf16 v[16:19], v[56:59], v[48:51], 0\n\t" \
    "ds_read_b128 v[100:103], v117\n\t" \
    "ds_read_b64_tr_b16 v[104:105], v118 offset:2048\n\t" \
    "v_mfma_f32_16x16x32_bf16 v[20:23], v[60:63], v[48:51], 0\n\t" \
    "ds_read_b64_tr_b16 v[106:107], v118 offset:2304\n\t" \
    "ds_read_b64_tr_b16 v[108:109], v119 offset:2048\n\t" \
    "v_mfma_f32_16x16x32_bf16 v[24:27], v[56:59], v[52:55], 0\n\t" \
    "ds_read_b64_tr_b16 v[110:111], v119 offset:2304\n\t" \
    "s_add_u32 %[slot], %[slot], 40960\n\t" \
    "s_cmp_eq_u32 %[slot], 122880\n\t" \
    "s_cselect_b32 %[slot], 0, %[slot]\n\t" \
    "v_mfma_f32_16x16x32_bf16 v[28:31], v[60:63], v[52:55], 0\n\t" \
    "v_add_u32 v112, %[slot], %[xa0]\n\t" \
    "v_add_u32 v113, %[slot], %[xa1]\n\t" \
    "v_mfma_f32_16x16x32_bf16 v[32:35], v[64:67], v[68:71], 0\n\t" \
    "v_add_u32 v114, %[slot], %[wa0]\n\t" \
    "v_add_u32 v115, %[slot], %[wa1]\n\t" \
    "v_mfma_f32_16x16x32_bf16 v[36:39], v[64:67], v[72:75], 0\n\t" \
    "v_add_u32 v116, %[xe], v112\n\t" \
    "v_add_u32 v117, %[xe], v113\n\t" \
    "v_add_u32 v118, %[slot], %[ea0]\n\t" \
    "v_add_u32 v119, %[slot], %[ea1]\n\t" \
    "s_branch 6f\n\t" \
    ".p2align 6\n\t" \
    "1:\n\t" \
    "s_waitcnt lgkmcnt(0)\n\t" \
    "v_mfma_f32_16x16x32_bf16 v[0:3], v[56:59], v[40:43], v[0:3]\n\t" \
    "ds_read_b128 v[76:79], v113 offset:0\n\t" \
    "ds_read_b64_tr_b16 v[92:93], v114 offset:4096\n\t" \
    "v_mfma_f32_16x16x32_bf16 v[4:7], v[60:63], v[40:43], v[4:7]\n\t" \
    "ds_read_b64_tr_b16 v[94:95], v114 offset:4608\n\t" \
    "ds_read_b64_tr_b16 v[96:97], v115 offset:4096\n\t" \
    "v_mfma_f32_16x16x32_bf16 v[8:11], v[56:59], v[44:47], v[8:11]\n\t" \
    "ds_read_b64_tr_b16 v[98:99], v115 offset:4608\n\t" \
    "ds_read_b128 v[80:83], v113 offset:2048\n\t" \
    "v_mfma_f32_16x16x32_bf16 v[12:15], v[60:63], v[44:47], v[12:15]\n\t" \
    "ds_read_b128 v[84:87], v113 offset:4096\n\t" \
    "ds_read_b128 v[88:91], v113 offset:6144\n\t" \
    "v_mfma_f32_16x16x32_bf16 v[16:19], v[56:59], v[48:51], v[16:19]\n\t" \
    "ds_read_b128 v[100:103], v117\n\t" \
    "ds_read_b64_tr_b16 v[104:105], v118 offset:2048\n\t" \
    "v_mfma_f32_16x16x32_bf16 v[20:23], v[60:63], v[48:51], v[20:23]\n\t" \
    "ds_read_b64_tr_b16 v[106:107], v118 offset:2304\n\t" \
    "ds_read_b64_tr_b16 v[108:109], v119 offset:2048\n\t" \
    "v_mfma_f32_16x16x32_bf16 v[24:27], v[56:59], v[52:55], v[24:27]\n\t" \
    "ds_read_b64_tr_b16 v[110:111], v119 offset:2304\n\t" \
    "s_add_u32 %[slot], %[slot], 40960\n\t" \
    "s_cmp_eq_u32 %[slot], 122880\n\t" \
    "s_cselect_b32 %[slot], 0, %[slot]\n\t" \
    "v_mfma_f32_16x16x32_bf16 v[28:31], v[60:63], v[52:55], v[28:31]\n\t" \
    "v_add_u32 v112, %[slot], %[xa0]\n\t" \
    "v_add_u32 v113, %[slot], %[xa1]\n\t" \
    "v_mfma_f32_16x16x32_bf16 v[32:35], v[64:67], v[68:71], v[32:35]\n\t" \
    "v_add_u32 v114, %[slot], %[wa0]\n\t" \
    "v_add_u32 v115, %[slot], %[wa1]\n\t" \
    "v_mfma_f32_16x16x32_bf16 v[36:39], v[64:67], v[72:75], v[36:39]\n\t" \
    "v_add_u32 v116, %[xe], v112\n\t" \
    "v_add_u32 v117, %[xe], v113\n\t" \
    "v_add_u32 v118, %[slot], %[ea0]\n\t" \
    "v_add_u32 v119, %[slot], %[ea1]\n\t" \
    "6:\n\t" \
    "s_waitcnt lgkmcnt(0)\n\t" \
    "v_mfma_f32_16x16x32_bf16 v[0:3], v[92:95], v[76:79], v[0:3]\n\t" \
    "v_mfma_f32_16x16x32_bf16 v[4:7], v[96:99], v[76:79], v[4:7]\n\t" \
    "s_barrier\n\t" \
    "ds_read_b128 v[40:43], v112 offset:0\n\t" \
    "ds_read_b64_tr_b16 v[56:57], v114 offset:0\n\t" \
    "v_mfma_f32_16x16x32_bf16 v[8:11], v[92:95], v[80:83], v[8:11]\n\t" \
    "ds_read_b64_tr_b16 v[58:59], v114 offset:512\n\t" \
    "ds_read_b64_tr_b16 v[60:61], v115 offset:0\n\t" \
    "v_mfma_f32_16x16x32_bf16 v[12:15], v[96:99], v[80:83], v[12:15]\n\t" \
    "ds_read_b64_tr_b16 v[62:63], v115 offset:512\n\t" \
    "ds_read_b128 v[44:47], v112 offset:2048\n\t" \
    "v_mfma_f32_16x16x32_bf16 v[16:19], v[92:95], v[84:87], v[16:19]\n\t" \
    "ds_read_b128 v[48:51], v112 offset:4096\n\t" \
    "ds_read_b128 v[52:55], v112 offset:6144\n\t" \
    "v_mfma_f32_16x16x32_bf16 v[20:23], v[96:99], v[84:87], v[20:23]\n\t" \
    "ds_read_b128 v[64:67], v116\n\t" \
    "ds_read_b64_tr_b16 v[68:69], v118 offset:0\n\t" \
    "v_mfma_f32_16x16x32_bf16 v[24:27], v[92:95], v[88:91], v[24:27]\n\t" \
    "ds_read_b64_tr_b16 v[70:71], v118 offset:256\n\t" \
    "ds_read_b64_tr_b16 v[72:73], v119 offset:0\n\t" \
    "v_mfma_f32_16x16x32_bf16 v[28:31], v[96:99], v[88:91], v[28:31]\n\t" \
    "ds_read_b64_tr_b16 v[74:75], v119 offset:256\n\t" \
    "s_sub_u32 %[cnt], %[cnt], 1\n\t" \
    "s_cmp_lg_u32 %[cnt], 0\n\t" \
    "v_mfma_f32_16x16x32_bf16 v[32:35], v[100:103], v[104:107], v[32:35]\n\t" \
    "v_mfma_f32_16x16x32_bf16 v[36:39], v[100:103], v[108:111], v[36:39]\n\t" \
    "s_cbranch_scc1 1b\n\t" \
    "s_waitcnt lgkmcnt(0)\n\t" \
    "s_nop 7\n\t"
#define GBV_CLOB "v40", "v41", "v42", "v43", "v44", "v45", "v46", "v47", "v48", "v49", "v50", "v51", "v52", "v53", "v54", "v55", "v56", "v57", "v58", "v59", "v60", "v61", "v62", "v63", "v64", "v65", "v66", "v67", "v68", "v69", "v70", "v71", "v72", "v73", "v74", "v75", "v76", "v77", "v78", "v79", "v80", "v81", "v82", "v83", "v84", "v85", "v86", "v87", "v88", "v89", "v90", "v91", "v92", "v93", "v94", "v95", "v96", "v97", "v98", "v99", "v100", "v101", "v102", "v103", "v104", "v105", "v106", "v107", "v108", "v109", "v110", "v111", "v112", "v113", "v114", "v115", "v116", "v117", "v118", "v119"
#define GA_ASM \
    "s_mov_b32 %[cnt], 16\n\t" \
    "v_add_u32 v176, %[slot], %[xa0]\n\t" \
    "v_add_u32 v177, %[slot], %[xa1]\n\t" \
    "v_add_u32 v178, %[slot], %[wa0]\n\t" \
    "v_add_u32 v179, %[slot], %[wa1]\n\t" \
    "ds_read_b128 v[96:99], v176 offset:0\n\t" \
    "ds_read_b64_tr_b16 v[112:113], v178 offset:0\n\t" \
    "ds_read_b64_tr_b16 v[114:115], v178 offset:512\n\t" \
    "ds_read_b64_tr_b16 v[116:117], v179 offset:0\n\t" \
    "ds_read_b64_tr_b16 v[118:119], v179 offset:512\n\t" \
    "ds_read_b64_tr_b16 v[120:121], v178 offset:8192\n\t" \
    "ds_read_b64_tr_b16 v[122:123], v178 offset:8704\n\t" \
    "ds_read_b64_tr_b16 v[124:125], v179 offset:8192\n\t" \
    "ds_read_b64_tr_b16 v[126:127], v179 offset:8704\n\t" \
    "ds_read_b64_tr_b16 v[128:129], v178 offset:16384\n\t" \
    "ds_read_b64_tr_b16 v[130:131], v178 offset:16896\n\t" \
    "ds_read_b64_tr_b16 v[132:133], v179 offset:16384\n\t" \
    "ds_read_b64_tr_b16 v[134:135], v179 offset:16896\n\t" \
    "ds_read_b128 v[100:103], v176 offset:2048\n\t" \
    "ds_read_b128 v[104:107], v176 offset:4096\n\t" \
    "ds_read_b128 v[108:111], v176 offset:6144\n\t" \
    "v_pk_mul_f32 v[0:1], v[0:1], %[qs] op_sel_hi:[1,0]\n\t" \
    "v_pk_mul_f32 v[2:3], v[2:3], %[qs] op_sel_hi:[1,0]\n\t" \
    "v_pk_mul_f32 v[4:5], v[4:5], %[qs] op_sel_hi:[1,0]\n\t" \
    "v_pk_mul_f32 v[6:7], v[6:7], %[qs] op_sel_hi:[1,0]\n\t" \
    "v_pk_mul_f32 v[8:9], v[8:9], %[qs] op_sel_hi:[1,0]\n\t" \
    "v_pk_mul_f32 v[10:11], v[10:11], %[qs] op_sel_hi:[1,0]\n\t" \
    "v_pk_mul_f32 v[12:13], v[12:13], %[qs] op_sel_hi:[1,0]\n\t" \
    "v_pk_mul_f32 v[14:15], v[14:15], %[qs] op_sel_hi:[1,0]\n\t" \
    "v_pk_mul_f32 v[16:17], v[16:17], %[qs] op_sel_hi:[1,0]\n\t" \
    "v_pk_mul_f32 v[18:19], v[18:19], %[qs] op_sel_hi:[1,0]\n\t" \
    "v_pk_mul_f32 v[20:21], v[20:21], %[qs] op_sel_hi:[1,0]\n\t" \
    "v_pk_mul_f32 v[22:23], v[22:23], %[qs] op_sel_hi:[1,0]\n\t" \
    "v_pk_mul_f32 v[24:25], v[24:25], %[qs] op_sel_hi:[1,0]\n\t" \
    "v_pk_mul_f32 v[26:27], v[26:27], %[qs] op_sel_hi:[1,0]\n\t" \
    "v_pk_mul_f32 v[28:29], v[28:29], %[qs] op_sel_hi:[1,0]\n\t" \
    "v_pk_mul_f32 v[30:31], v[30:31], %[qs] op_sel_hi:[1,0]\n\t" \
    "v_cvt_pk_bf16_f32 v200, v0, v1\n\t" \
    "v_cvt_pk_bf16_f32 v201, v2, v3\n\t" \
    "v_cvt_pk_bf16_f32 v202, v4, v5\n\t" \
    "v_cvt_pk_bf16_f32 v203, v6, v7\n\t" \
    "v_cvt_pk_bf16_f32 v204, v8, v9\n\t" \
    "v_cvt_pk_bf16_f32 v205, v10, v11\n\t" \
    "v_cvt_pk_bf16_f32 v206, v12, v13\n\t" \
    "v_cvt_pk_bf16_f32 v207, v14, v15\n\t" \
    "v_cvt_pk_bf16_f32 v208, v16, v17\n\t" \
    "v_cvt_pk_bf16_f32 v209, v18, v19\n\t" \
    "v_cvt_pk_bf16_f32 v210, v20, v21\n\t" \
    "v_cvt_pk_bf16_f32 v211, v22, v23\n\t" \
    "v_cvt_pk_bf16_f32 v212, v24, v25\n\t" \
    "v_cvt_pk_bf16_f32 v213, v26, v27\n\t" \
    "v_cvt_pk_bf16_f32 v214, v28, v29\n\t" \
    "v_cvt_pk_bf16_f32 v215, v30, v31\n\t" \
    "v_permlane16_swap_b32 v200, v204\n\t" \
    "v_permlane16_swap_b32 v201, v205\n\t" \
    "v_permlane16_swap_b32 v202, v206\n\t" \
    "v_permlane16_swap_b32 v203, v207\n\t" \
    "v_permlane16_swap_b32 v208, v212\n\t" \
    "v_permlane16_swap_b32 v209, v213\n\t" \
    "v_permlane16_swap_b32 v210, v214\n\t" \
    "v_permlane16_swap_b32 v211, v215\n\t" \
    "v_permlane32_swap_b32 v200, v204\n\t" \
    "v_permlane32_swap_b32 v201, v205\n\t" \
    "v_permlane32_swap_b32 v202, v206\n\t" \
    "v_permlane32_swap_b32 v203, v207\n\t" \
    "v_permlane32_swap_b32 v208, v212\n\t" \
    "v_permlane32_swap_b32 v209, v213\n\t" \
    "v_permlane32_swap_b32 v210, v214\n\t" \
    "v_permlane32_swap_b32 v211, v215\n\t" \
    "s_waitcnt lgkmcnt(0)\n\t" \
    "v_mfma_f32_16x16x32_bf16 v[0:3], v[112:115], v[96:99], 0\n\t" \
    "ds_read_b128 v[136:139], v177 offset:0\n\t" \
    "v_mfma_f32_16x16x32_bf16 v[4:7], v[116:119], v[96:99], 0\n\t" \
    "ds_read_b64_tr_b16 v[152:153], v178 offset:4096\n\t" \
    "v_mfma_f32_16x16x32_bf16 v[8:11], v[96:99], v[120:123], 0\n\t" \
    "ds_read_b64_tr_b16 v[154:155], v178 offset:4608\n\t" \
    "v_mfma_f32_16x16x32_bf16 v[12:15], v[96:99], v[124:127], 0\n\t" \
    "ds_read_b64_tr_b16 v[156:157], v179 offset:4096\n\t" \
    "v_mfma_f32_16x16x32_bf16 v[16:19], v[96:99], v[128:131], 0\n\t" \
    "ds_read_b64_tr_b16 v[158:159], v179 offset:4608\n\t" \
    "v_mfma_f32_16x16x32_bf16 v[20:23], v[96:99], v[132:135], 0\n\t" \
    "ds_read_b64_tr_b16 v[160:161], v178 offset:12288\n\t" \
    "v_mfma_f32_16x16x32_bf16 v[24:27], v[112:115], v[100:103], 0\n\t" \
    "ds_read_b64_tr_b16 v[162:163], v178 offset:12800\n\t" \
    "v_mfma_f32_16x16x32_bf16 v[28:31], v[116:119], v[100:103], 0\n\t" \
    "ds_read_b64_tr_b16 v[164:165], v179 offset:12288\n\t" \
    "v_mfma_f32_16x16x32_bf16 v[32:35], v[100:103], v[120:123], 0\n\t" \
    "ds_read_b64_tr_b16 v[166:167], v179 offset:12800\n\t" \
    "v_mfma_f32_16x16x32_bf16 v[36:39], v[100:103], v[124:127], 0\n\t" \
    "ds_read_b64_tr_b16 v[168:169], v178 offset:20480\n\t" \
    "v_mfma_f32_16x16x32_bf16 v[40:43], v[100:103], v[128:131], 0\n\t" \
    "ds_read_b64_tr_b16 v[170:171], v178 offset:20992\n\t" \
    "v_mfma_f32_16x16x32_bf16 v[44:47], v[100:103], v[132:135], 0\n\t" \
    "ds_read_b64_tr_b16 v[172:173], v179 offset:20480\n\t" \
    "v_mfma_f32_16x16x32_bf16 v[48:51], v[112:115], v[104:107], 0\n\t" \
    "ds_read_b64_tr_b16 v[174:175], v179 offset:20992\n\t" \
    "v_mfma_f32_16x16x32_bf16 v[52:55], v[116:119], v[104:107], 0\n\t" \
    "ds_read_b128 v[140:143], v177 offset:2048\n\t" \
    "v_mfma_f32_16x16x32_bf16 v[56:59], v[104:107], v[120:123], 0\n\t" \
    "ds_read_b128 v[144:147], v177 offset:4096\n\t" \
    "v_mfma_f32_16x16x32_bf16 v[60:63], v[104:107], v[124:127], 0\n\t" \
    "ds_read_b128 v[148:151], v177 offset:6144\n\t" \
    "v_mfma_f32_16x16x32_bf16 v[64:67], v[104:107], v[128:131], 0\n\t" \
    "s_add_u32 %[slot], %[slot], 40960\n\t" \
    "s_cmp_eq_u32 %[slot], 122880\n\t" \
    "s_cselect_b32 %[slot], 0, %[slot]\n\t" \
    "v_mfma_f32_16x16x32_bf16 v[68:71], v[104:107], v[132:135], 0\n\t" \
    "v_add_u32 v176, %[slot], %[xa0]\n\t" \
    "v_mfma_f32_16x16x32_bf16 v[72:75], v[112:115], v[108:111], 0\n\t" \
    "v_add_u32 v177, %[slot], %[xa1]\n\t" \
    "v_mfma_f32_16x16x32_bf16 v[76:79], v[116:119], v[108:111], 0\n\t" \
    "v_add_u32 v178, %[slot], %[wa0]\n\t" \
    "v_mfma_f32_16x16x32_bf16 v[80:83], v[108:111], v[120:123], 0\n\t" \
    "v_add_u32 v179, %[slot], %[wa1]\n\t" \
    "v_mfma_f32_16x16x32_bf16 v[84:87], v[108:111], v[124:127], 0\n\t" \
    "v_mfma_f32_16x16x32_bf16 v[88:91], v[108:111], v[128:131], 0\n\t" \
    "v_mfma_f32_16x16x32_bf16 v[92:95], v[108:111], v[132:135], 0\n\t" \
    "s_branch 6f\n\t" \
    ".p2align 6\n\t" \
    "1:\n\t" \
    "s_waitcnt lgkmcnt(0)\n\t" \
    "v_mfma_f32_16x16x32_bf16 v[0:3], v[112:115], v[96:99], v[0:3]\n\t" \
    "ds_read_b128 v[136:139], v177 offset:0\n\t" \
    "v_mfma_f32_16x16x32_bf16 v[4:7], v[116:119], v[96:99], v[4:7]\n\t" \
    "ds_read_b64_tr_b16 v[152:153], v178 offset:4096\n\t" \
    "v_mfma_f32_16x16x32_bf16 v[8:11], v[96:99], v[120:123], v[8:11]\n\t" \
    "ds_read_b64_tr_b16 v[154:155], v178 offset:4608\n\t" \
    "v_mfma_f32_16x16x32_bf16 v[12:15], v[96:99], v[124:127], v[12:15]\n\t" \
    "ds_read_b64_tr_b16 v[156:157], v179 offset:4096\n\t" \
    "v_mfma_f32_16x16x32_bf16 v[16:19], v[96:99], v[128:131], v[16:19]\n\t" \
    "ds_read_b64_tr_b16 v[158:159], v179 offset:4608\n\t" \
    "v_mfma_f32_16x16x32_bf16 v[20:23], v[96:99], v[132:135], v[20:23]\n\t" \
    "ds_read_b64_tr_b16 v[160:161], v178 offset:12288\n\t" \
    "v_mfma_f32_16x16x32_bf16 v[24:27], v[112:115], v[100:103], v[24:27]\n\t" \
    "ds_read_b64_tr_b16 v[162:163], v178 offset:12800\n\t" \
    "v_mfma_f32_16x16x32_bf16 v[28:31], v[116:119], v[100:103], v[28:31]\n\t" \
    "ds_read_b64_tr_b16 v[164:165], v179 offset:12288\n\t" \
    "v_mfma_f32_16x16x32_bf16 v[32:35], v[100:103], v[120:123], v[32:35]\n\t" \
    "ds_read_b64_tr_b16 v[166:167], v179 offset:12800\n\t" \
    "v_mfma_f32_16x16x32_bf16 v[36:39], v[100:103], v[124:127], v[36:39]\n\t" \
    "ds_read_b64_tr_b16 v[168:169], v178 offset:20480\n\t" \
    "v_mfma_f32_16x16x32_bf16 v[40:43], v[100:103], v[128:131], v[40:43]\n\t" \
    "ds_read_b64_tr_b16 v[170:171], v178 offset:20992\n\t" \
    "v_mfma_f32_16x16x32_bf16 v[44:47], v[100:103], v[132:135], v[44:47]\n\t" \
    "ds_read_b64_tr_b16 v[172:173], v179 offset:20480\n\t" \
    "v_mfma_f32_16x16x32_bf16 v[48:51], v[112:115], v[104:107], v[48:51]\n\t" \
    "ds_read_b64_tr_b16 v[174:175], v179 offset:20992\n\t" \
    "v_mfma_f32_16x16x32_bf16 v[52:55], v[116:119], v[104:107], v[52:55]\n\t" \
    "ds_read_b128 v[140:143], v177 offset:2048\n\t" \
    "v_mfma_f32_16x16x32_bf16 v[56:59], v[104:107], v[120:123], v[56:59]\n\t" \
    "ds_read_b128 v[144:147], v177 offset:4096\n\t" \
    "v_mfma_f32_16x16x32_bf16 v[60:63], v[104:107], v[124:127], v[60:63]\n\t" \
    "ds_read_b128 v[148:151], v177 offset:6144\n\t" \
    "v_mfma_f32_16x16x32_bf16 v[64:67], v[104:107], v[128:131], v[64:67]\n\t" \
    "s_add_u32 %[slot], %[slot], 40960\n\t" \
    "s_cmp_eq_u32 %[slot], 122880\n\t" \
    "s_cselect_b32 %[slot], 0, %[slot]\n\t" \
    "v_mfma_f32_16x16x32_bf16 v[68:71], v[104:107], v[132:135], v[68:71]\n\t" \
    "v_add_u32 v176, %[slot], %[xa0]\n\t" \
    "v_mfma_f32_16x16x32_bf16 v[72:75], v[112:115], v[108:111], v[72:75]\n\t" \
    "v_add_u32 v177, %[slot], %[xa1]\n\t" \
    "v_mfma_f32_16x16x32_bf16 v[76:79], v[116:119], v[108:111], v[76:79]\n\t" \
    "v_add_u32 v178, %[slot], %[wa0]\n\t" \
    "v_mfma_f32_16x16x32_bf16 v[80:83], v[108:111], v[120:123], v[80:83]\n\t" \
    "v_add_u32 v179, %[slot], %[wa1]\n\t" \
    "v_mfma_f32_16x16x32_bf16 v[84:87], v[108:111], v[124:127], v[84:87]\n\t" \
    "s_cmp_lg_u32 %[cnt], %[pubc]\n\t" \
    "s_cbranch_scc1 3f\n\t" \
    "s_waitcnt vmcnt(0)\n\t" \
    "global_store_dwordx2 %[zero], %[tag], %[fa] sc1\n\t" \
    "3:\n\t" \
    "v_mfma_f32_16x16x32_bf16 v[88:91], v[108:111], v[128:131], v[88:91]\n\t" \
    "v_mfma_f32_16x16x32_bf16 v[92:95], v[108:111], v[132:135], v[92:95]\n\t" \
    "6:\n\t" \
    "s_waitcnt lgkmcnt(0)\n\t" \
    "v_mfma_f32_16x16x32_bf16 v[0:3], v[152:155], v[136:139], v[0:3]\n\t" \
    "v_mfma_f32_16x16x32_bf16 v[4:7], v[156:159], v[136:139], v[4:7]\n\t" \
    "s_barrier\n\t" \
    "ds_read_b128 v[96:99], v176 offset:0\n\t" \
    "v_mfma_f32_16x16x32_bf16 v[8:11], v[136:139], v[160:163], v[8:11]\n\t" \
    "ds_read_b64_tr_b16 v[112:113], v178 offset:0\n\t" \
    "v_mfma_f32_16x16x32_bf16 v[12:15], v[136:139], v[164:167], v[12:15]\n\t" \
    "ds_read_b64_tr_b16 v[114:115], v178 offset:512\n\t" \
    "v_mfma_f32_16x16x32_bf16 v[16:19], v[136:139], v[168:171], v[16:19]\n\t" \
    "ds_read_b64_tr_b16 v[116:117], v179 offset:0\n\t" \
    "v_mfma_f32_16x16x32_bf16 v[20:23], v[136:139], v[172:175], v[20:23]\n\t" \
    "ds_read_b64_tr_b16 v[118:119], v179 offset:512\n\t" \
    "v_mfma_f32_16x16x32_bf16 v[24:27], v[152:155], v[140:143], v[24:27]\n\t" \
    "ds_read_b64_tr_b16 v[120:121], v178 offset:8192\n\t" \
    "v_mfma_f32_16x16x32_bf16 v[28:31], v[156:159], v[140:143], v[28:31]\n\t" \
    "ds_read_b64_tr_b16 v[122:123], v178 offset:8704\n\t" \
    "v_mfma_f32_16x16x32_bf16 v[32:35], v[140:143], v[160:163], v[32:35]\n\t" \
    "ds_read_b64_tr_b16 v[124:125], v179 offset:8192\n\t" \
    "v_mfma_f32_16x16x32_bf16 v[36:39], v[140:143], v[164:167], v[36:39]\n\t" \
    "ds_read_b64_tr_b16 v[126:127], v179 offset:8704\n\t" \
    "v_mfma_f32_16x16x32_bf16 v[40:43], v[140:143], v[168:171], v[40:43]\n\t" \
    "ds_read_b64_tr_b16 v[128:129], v178 offset:16384\n\t" \
    "v_mfma_f32_16x16x32_bf16 v[44:47], v[140:143], v[172:175], v[44:47]\n\t" \
    "ds_read_b64_tr_b16 v[130:131], v178 offset:16896\n\t" \
    "v_mfma_f32_16x16x32_bf16 v[48:51], v[152:155], v[144:147], v[48:51]\n\t" \
    "ds_read_b64_tr_b16 v[132:133], v179 offset:16384\n\t" \
    "v_mfma_f32_16x16x32_bf16 v[52:55], v[156:159], v[144:147], v[52:55]\n\t" \
    "ds_read_b64_tr_b16 v[134:135], v179 offset:16896\n\t" \
    "v_mfma_f32_16x16x32_bf16 v[56:59], v[144:147], v[160:163], v[56:59]\n\t" \
    "ds_read_b128 v[100:103], v176 offset:2048\n\t" \
    "v_mfma_f32_16x16x32_bf16 v[60:63], v[144:147], v[164:167], v[60:63]\n\t" \
    "ds_read_b128 v[104:107], v176 offset:4096\n\t" \
    "v_mfma_f32_16x16x32_bf16 v[64:67], v[144:147], v[168:171], v[64:67]\n\t" \
    "ds_read_b128 v[108:111], v176 offset:6144\n\t" \
    "v_mfma_f32_16x16x32_bf16 v[68:71], v[144:147], v[172:175], v[68:71]\n\t" \
    "s_sub_u32 %[cnt], %[cnt], 1\n\t" \
    "s_cmp_lg_u32 %[cnt], 0\n\t" \
    "v_mfma_f32_16x16x32_bf16 v[72:75], v[152:155], v[148:151], v[72:75]\n\t" \
    "v_mfma_f32_16x16x32_bf16 v[76:79], v[156:159], v[148:151], v[76:79]\n\t" \
    "v_mfma_f32_16x16x32_bf16 v[80:83], v[148:151], v[160:163], v[80:83]\n\t" \
    "v_mfma_f32_16x16x32_bf16 v[84:87], v[148:151], v[164:167], v[84:87]\n\t" \
    "v_mfma_f32_16x16x32_bf16 v[88:91], v[148:151], v[168:171], v[88:91]\n\t" \
    "v_mfma_f32_16x16x32_bf16 v[92:95], v[148:151], v[172:175], v[92:95]\n\t" \
    "s_cbranch_scc1 1b\n\t" \
    "s_waitcnt lgkmcnt(0)\n\t" \
    "s_nop 7\n\t"
#define GA_CLOB "v96", "v97", "v98", "v99", "v100", "v101", "v102", "v103", "v104", "v105", "v106", "v107", "v108", "v109", "v110", "v111", "v112", "v113", "v114", "v115", "v116", "v117", "v118", "v119", "v120", "v121", "v122", "v123", "v124", "v125", "v126", "v127", "v128", "v129", "v130", "v131", "v132", "v133", "v134", "v135", "v136", "v137", "v138", "v139", "v140", "v141", "v142", "v143", "v144", "v145", "v146", "v147", "v148", "v149", "v150", "v151", "v152", "v153", "v154", "v155", "v156", "v157", "v158", "v159", "v160", "v161", "v162", "v163", "v164", "v165", "v166", "v167", "v168", "v169", "v170", "v171", "v172", "v173", "v174", "v175", "v176", "v177", "v178", "v179"
#define GO_ASM \
    "s_mov_b32 %[cnt], 16\n\t" \
    "v_add_u32 v128, %[slot], %[xa0]\n\t" \
    "v_add_u32 v129, %[slot], %[wa0]\n\t" \
    "v_add_u32 v130, %[slot], %[wa1]\n\t" \
    "v_add_u32 v131, %[slot], %[wa2]\n\t" \
    "v_add_u32 v132, %[slot], %[wa3]\n\t" \
    "s_barrier\n\t" \
    "ds_read_b128 v[64:67], v128 offset:0\n\t" \
    "ds_read_b64_tr_b16 v[80:81], v129 offset:0\n\t" \
    "ds_read_b64_tr_b16 v[82:83], v129 offset:1024\n\t" \
    "ds_read_b64_tr_b16 v[84:85], v130 offset:0\n\t" \
    "ds_read_b64_tr_b16 v[86:87], v130 offset:1024\n\t" \
    "ds_read_b64_tr_b16 v[88:89], v131 offset:0\n\t" \
    "ds_read_b64_tr_b16 v[90:91], v131 offset:1024\n\t" \
    "ds_read_b64_tr_b16 v[92:93], v132 offset:0\n\t" \
    "ds_read_b64_tr_b16 v[94:95], v132 offset:1024\n\t" \
    "ds_read_b128 v[68:71], v128 offset:256\n\t" \
    "ds_read_b128 v[72:75], v128 offset:4096\n\t" \
    "ds_read_b128 v[76:79], v128 offset:4352\n\t" \
    "s_waitcnt lgkmcnt(0)\n\t" \
    "v_mfma_f32_16x16x32_bf16 v[0:3], v[80:83], v[64:67], 0\n\t" \
    "ds_read_b128 v[96:99], v128 offset:2048\n\t" \
    "v_mfma_f32_16x16x32_bf16 v[4:7], v[84:87], v[64:67], 0\n\t" \
    "ds_read_b64_tr_b16 v[112:113], v129 offset:4096\n\t" \
    "v_mfma_f32_16x16x32_bf16 v[8:11], v[88:91], v[64:67], 0\n\t" \
    "ds_read_b64_tr_b16 v[114:115], v129 offset:5120\n\t" \
    "v_mfma_f32_16x16x32_bf16 v[12:15], v[92:95], v[64:67], 0\n\t" \
    "ds_read_b64_tr_b16 v[116:117], v130 offset:4096\n\t" \
    "v_mfma_f32_16x16x32_bf16 v[16:19], v[80:83], v[68:71], 0\n\t" \
    "ds_read_b64_tr_b16 v[118:119], v130 offset:5120\n\t" \
    "v_mfma_f32_16x16x32_bf16 v[20:23], v[84:87], v[68:71], 0\n\t" \
    "ds_read_b64_tr_b16 v[120:121], v131 offset:4096\n\t" \
    "v_mfma_f32_16x16x32_bf16 v[24:27], v[88:91], v[68:71], 0\n\t" \
    "ds_read_b64_tr_b16 v[122:123], v131 offset:5120\n\t" \
    "v_mfma_f32_16x16x32_bf16 v[28:31], v[92:95], v[68:71], 0\n\t" \
    "ds_read_b64_tr_b16 v[124:125], v132 offset:4096\n\t" \
    "v_mfma_f32_16x16x32_bf16 v[32:35], v[80:83], v[72:75], 0\n\t" \
    "ds_read_b64_tr_b16 v[126:127], v132 offset:5120\n\t" \
    "v_mfma_f32_16x16x32_bf16 v[36:39], v[84:87], v[72:75], 0\n\t" \
    "ds_read_b128 v[100:103], v128 offset:2304\n\t" \
    "v_mfma_f32_16x16x32_bf16 v[40:43], v[88:91], v[72:75], 0\n\t" \
    "ds_read_b128 v[104:107], v128 offset:6144\n\t" \
    "v_mfma_f32_16x16x32_bf16 v[44:47], v[92:95], v[72:75], 0\n\t" \
    "ds_read_b128 v[108:111], v128 offset:6400\n\t" \
    "v_mfma_f32_16x16x32_bf16 v[48:51], v[80:83], v[76:79], 0\n\t" \
    "s_add_u32 %[slot], %[slot], 32768\n\t" \
    "s_and_b32 %[slot], %[slot], 131071\n\t" \
    "v_add_u32 v128, %[slot], %[xa0]\n\t" \
    "v_mfma_f32_16x16x32_bf16 v[52:55], v[84:87], v[76:79], 0\n\t" \
    "v_add_u32 v129, %[slot], %[wa0]\n\t" \
    "v_add_u32 v130, %[slot], %[wa1]\n\t" \
    "v_mfma_f32_16x16x32_bf16 v[56:59], v[88:91], v[76:79], 0\n\t" \
    "v_add_u32 v131, %[slot], %[wa2]\n\t" \
    "v_add_u32 v132, %[slot], %[wa3]\n\t" \
    "v_mfma_f32_16x16x32_bf16 v[60:63], v[92:95], v[76:79], 0\n\t" \
    "s_branch 6f\n\t" \
    ".p2align 6\n\t" \
    "1:\n\t" \
    "s_waitcnt lgkmcnt(0)\n\t" \
    "v_mfma_f32_16x16x32_bf16 v[0:3], v[80:83], v[64:67], v[0:3]\n\t" \
    "ds_read_b128 v[96:99], v128 offset:2048\n\t" \
    "v_mfma_f32_16x16x32_bf16 v[4:7], v[84:87], v[64:67], v[4:7]\n\t" \
    "ds_read_b64_tr_b16 v[112:113], v129 offset:4096\n\t" \
    "v_mfma_f32_16x16x32_bf16 v[8:11], v[88:91], v[64:67], v[8:11]\n\t" \
    "ds_read_b64_tr_b16 v[114:115], v129 offset:5120\n\t" \
    "v_mfma_f32_16x16x32_bf16 v[12:15], v[92:95], v[64:67], v[12:15]\n\t" \
    "ds_read_b64_tr_b16 v[116:117], v130 offset:4096\n\t" \
    "v_mfma_f32_16x16x32_bf16 v[16:19], v[80:83], v[68:71], v[16:19]\n\t" \
    "ds_read_b64_tr_b16 v[118:119], v130 offset:5120\n\t" \
    "v_mfma_f32_16x16x32_bf16 v[20:23], v[84:87], v[68:71], v[20:23]\n\t" \
    "ds_read_b64_tr_b16 v[120:121], v131 offset:4096\n\t" \
    "v_mfma_f32_16x16x32_bf16 v[24:27], v[88:91], v[68:71], v[24:27]\n\t" \
    "ds_read_b64_tr_b16 v[122:123], v131 offset:5120\n\t" \
    "v_mfma_f32_16x16x32_bf16 v[28:31], v[92:95], v[68:71], v[28:31]\n\t" \
    "ds_read_b64_tr_b16 v[124:125], v132 offset:4096\n\t" \
    "v_mfma_f32_16x16x32_bf16 v[32:35], v[80:83], v[72:75], v[32:35]\n\t" \
    "ds_read_b64_tr_b16 v[126:127], v132 offset:5120\n\t" \
    "v_mfma_f32_16x16x32_bf16 v[36:39], v[84:87], v[72:75], v[36:39]\n\t" \
    "ds_read_b128 v[100:103], v128 offset:2304\n\t" \
    "v_mfma_f32_16x16x32_bf16 v[40:43], v[88:91], v[72:75], v[40:43]\n\t" \
    "ds_read_b128 v[104:107], v128 offset:6144\n\t" \
    "v_mfma_f32_16x16x32_bf16 v[44:47], v[92:95], v[72:75], v[44:47]\n\t" \
    "ds_read_b128 v[108:111], v128 offset:6400\n\t" \
    "v_mfma_f32_16x16x32_bf16 v[48:51], v[80:83], v[76:79], v[48:51]\n\t" \
    "s_add_u32 %[slot], %[slot], 32768\n\t" \
    "s_and_b32 %[slot], %[slot], 131071\n\t" \
    "v_add_u32 v128, %[slot], %[xa0]\n\t" \
    "v_mfma_f32_16x16x32_bf16 v[52:55], v[84:87], v[76:79], v[52:55]\n\t" \
    "v_add_u32 v129, %[slot], %[wa0]\n\t" \
    "v_add_u32 v130, %[slot], %[wa1]\n\t" \
    "v_mfma_f32_16x16x32_bf16 v[56:59], v[88:91], v[76:79], v[56:59]\n\t" \
    "v_add_u32 v131, %[slot], %[wa2]\n\t" \
    "v_add_u32 v132, %[slot], %[wa3]\n\t" \
    "v_mfma_f32_16x16x32_bf16 v[60:63], v[92:95], v[76:79], v[60:63]\n\t" \
    "6:\n\t" \
    "s_waitcnt lgkmcnt(0)\n\t" \
    "v_mfma_f32_16x16x32_bf16 v[0:3], v[112:115], v[96:99], v[0:3]\n\t" \
    "v_mfma_f32_16x16x32_bf16 v[4:7], v[116:119], v[96:99], v[4:7]\n\t" \
    "s_barrier\n\t" \
    "ds_read_b128 v[64:67], v128 offset:0\n\t" \
    "v_mfma_f32_16x16x32_bf16 v[8:11], v[120:123], v[96:99], v[8:11]\n\t" \
    "ds_read_b64_tr_b16 v[80:81], v129 offset:0\n\t" \
    "v_mfma_f32_16x16x32_bf16 v[12:15], v[124:127], v[96:99], v[12:15]\n\t" \
    "ds_read_b64_tr_b16 v[82:83], v129 offset:1024\n\t" \
    "v_mfma_f32_16x16x32_bf16 v[16:19], v[112:115], v[100:103], v[16:19]\n\t" \
    "ds_read_b64_tr_b16 v[84:85], v130 offset:0\n\t" \
    "v_mfma_f32_16x16x32_bf16 v[20:23], v[116:119], v[100:103], v[20:23]\n\t" \
    "ds_read_b64_tr_b16 v[86:87], v130 offset:1024\n\t" \
    "v_mfma_f32_16x16x32_bf16 v[24:27], v[120:123], v[100:103], v[24:27]\n\t" \
    "ds_read_b64_tr_b16 v[88:89], v131 offset:0\n\t" \
    "v_mfma_f32_16x16x32_bf16 v[28:31], v[124:127], v[100:103], v[28:31]\n\t" \
    "ds_read_b64_tr_b16 v[90:91], v131 offset:1024\n\t" \
    "v_mfma_f32_16x16x32_bf16 v[32:35], v[112:115], v[104:107], v[32:35]\n\t" \
    "ds_read_b64_tr_b16 v[92:93], v132 offset:0\n\t" \
    "v_mfma_f32_16x16x32_bf16 v[36:39], v[116:119], v[104:107], v[36:39]\n\t" \
    "ds_read_b64_tr_b16 v[94:95], v132 offset:1024\n\t" \
    "v_mfma_f32_16x16x32_bf16 v[40:43], v[120:123], v[104:107], v[40:43]\n\t" \
    "ds_read_b128 v[68:71], v128 offset:256\n\t" \
    "v_mfma_f32_16x16x32_bf16 v[44:47], v[124:127], v[104:107], v[44:47]\n\t" \
    "ds_read_b128 v[72:75], v128 offset:4096\n\t" \
    "v_mfma_f32_16x16x32_bf16 v[48:51], v[112:115], v[108:111], v[48:51]\n\t" \
    "ds_read_b128 v[76:79], v128 offset:4352\n\t" \
    "v_mfma_f32_16x16x32_bf16 v[52:55], v[116:119], v[108:111], v[52:55]\n\t" \
    "s_sub_u32 %[cnt], %[cnt], 1\n\t" \
    "s_cmp_lg_u32 %[cnt], 0\n\t" \
    "v_mfma_f32_16x16x32_bf16 v[56:59], v[120:123], v[108:111], v[56:59]\n\t" \
    "v_mfma_f32_16x16x32_bf16 v[60:63], v[124:127], v[108:111], v[60:63]\n\t" \
    "s_cbranch_scc1 1b\n\t" \
    "s_waitcnt lgkmcnt(0)\n\t" \
    "s_nop 7\n\t"
#define GO_CLOB "v64", "v65", "v66", "v67", "v68", "v69", "v70", "v71", "v72", "v73", "v74", "v75", "v76", "v77", "v78", "v79", "v80", "v81", "v82", "v83", "v84", "v85", "v86", "v87", "v88", "v89", "v90", "v91", "v92", "v93", "v94", "v95", "v96", "v97", "v98", "v99", "v100", "v101", "v102", "v103", "v104", "v105", "v106", "v107", "v108", "v109", "v110", "v111", "v112", "v113", "v114", "v115", "v116", "v117", "v118", "v119", "v120", "v121", "v122", "v123", "v124", "v125", "v126", "v127", "v128", "v129", "v130", "v131", "v132"
#define ATTN_ASM \
    "ds_read_b128 v[136:139], %[ka]\n\t" \
    "ds_read_b128 v[140:143], %[ka] offset:1024\n\t" \
    "v_mov_b64 v[0:1], 0\n\t" \
    "v_mov_b64 v[2:3], 0\n\t" \
    "v_mov_b64 v[4:5], 0\n\t" \
    "v_mov_b64 v[6:7], 0\n\t" \
    "v_mov_b64 v[8:9], 0\n\t" \
    "v_mov_b64 v[10:11], 0\n\t" \
    "v_mov_b64 v[12:13], 0\n\t" \
    "v_mov_b64 v[14:15], 0\n\t" \
    "v_mov_b64 v[16:17], 0\n\t" \
    "v_mov_b64 v[18:19], 0\n\t" \
    "v_mov_b64 v[20:21], 0\n\t" \
    "v_mov_b64 v[22:23], 0\n\t" \
    "v_mov_b64 v[24:25], 0\n\t" \
    "v_mov_b64 v[26:27], 0\n\t" \
    "v_mov_b64 v[28:29], 0\n\t" \
    "v_mov_b64 v[30:31], 0\n\t" \
    "v_mov_b64 v[32:33], 0\n\t" \
    "v_mov_b64 v[34:35], 0\n\t" \
    "v_mov_b64 v[36:37], 0\n\t" \
    "v_mov_b64 v[38:39], 0\n\t" \
    "s_waitcnt lgkmcnt(0)\n\t" \
    "v_mfma_f32_32x32x16_bf16 v[72:87], v[136:139], %[qa0], 0\n\t" \
    "v_mfma_f32_32x32x16_bf16 v[104:119], v[136:139], %[qb0], 0\n\t" \
    "v_mfma_f32_32x32x16_bf16 v[72:87], v[140:143], %[qa1], v[72:87]\n\t" \
    "v_mfma_f32_32x32x16_bf16 v[104:119], v[140:143], %[qb1], v[104:119]\n\t" \
    "v_mov_b64 v[120:121], 0\n\t" \
    "v_mov_b64 v[122:123], 0\n\t" \
    "v_mov_b64 v[124:125], 0\n\t" \
    "v_mov_b64 v[126:127], 0\n\t" \
    "v_mov_b64 v[128:129], 0\n\t" \
    "v_mov_b64 v[130:131], 0\n\t" \
    "v_mov_b64 v[132:133], 0\n\t" \
    "v_mov_b64 v[134:135], 0\n\t" \
    "v_mov_b64 v[160:161], 0\n\t" \
    "v_mov_b64 v[162:163], 0\n\t" \
    "v_mov_b64 v[164:165], 0\n\t" \
    "v_mov_b64 v[166:167], 0\n\t" \
    "v_mov_b32 v168, %[lad]\n\t" \
    "s_mov_b32 %[cnt], 0\n\t" \
    "s_nop 1\n\t" \
    "v_max3_f32 v40, v72, v73, v74\n\t" \
    "v_max3_f32 v40, v40, v75, v76\n\t" \
    "v_max3_f32 v40, v40, v77, v78\n\t" \
    "v_max3_f32 v40, v40, v79, v80\n\t" \
    "v_max3_f32 v40, v40, v81, v82\n\t" \
    "v_max3_f32 v40, v40, v83, v84\n\t" \
    "v_max3_f32 v40, v40, v85, v86\n\t" \
    "v_max_f32 v40, v40, v87\n\t" \
    "v_mov_b32 v41, v40\n\t" \
    "v_max3_f32 v56, v104, v105, v106\n\t" \
    "v_max3_f32 v56, v56, v107, v108\n\t" \
    "v_max3_f32 v56, v56, v109, v110\n\t" \
    "v_max3_f32 v56, v56, v111, v112\n\t" \
    "v_max3_f32 v56, v56, v113, v114\n\t" \
    "v_max3_f32 v56, v56, v115, v116\n\t" \
    "v_max3_f32 v56, v56, v117, v118\n\t" \
    "v_max_f32 v56, v56, v119\n\t" \
    "v_mov_b32 v57, v56\n\t" \
    "s_nop 0\n\t" \
    "v_permlane32_swap_b32 v40, v41\n\t" \
    "v_permlane32_swap_b32 v56, v57\n\t" \
    "v_min_f32_e64 v40, -v40, -v41\n\t" \
    "v_min_f32_e64 v56, -v56, -v57\n\t" \
    "v_mov_b32 v41, v40\n\t" \
    "v_mov_b32 v57, v56\n\t" \
    "v_pk_add_f32 v[72:73], v[72:73], v[40:41]\n\t" \
    "v_pk_add_f32 v[74:75], v[74:75], v[40:41]\n\t" \
    "v_pk_add_f32 v[76:77], v[76:77], v[40:41]\n\t" \
    "v_pk_add_f32 v[78:79], v[78:79], v[40:41]\n\t" \
    "v_pk_add_f32 v[80:81], v[80:81], v[40:41]\n\t" \
    "v_pk_add_f32 v[82:83], v[82:83], v[40:41]\n\t" \
    "v_pk_add_f32 v[84:85], v[84:85], v[40:41]\n\t" \
    "v_pk_add_f32 v[86:87], v[86:87], v[40:41]\n\t" \
    "v_mov_b64 v[58:59], v[56:57]\n\t" \
    "v_mov_b64 v[42:43], v[40:41]\n\t" \
    "v_mov_b64 v[60:61], v[56:57]\n\t" \
    "v_mov_b64 v[44:45], v[40:41]\n\t" \
    "v_mov_b64 v[62:63], v[56:57]\n\t" \
    "v_mov_b64 v[46:47], v[40:41]\n\t" \
    "v_mov_b64 v[64:65], v[56:57]\n\t" \
    "v_mov_b64 v[48:49], v[40:41]\n\t" \
    "v_mov_b64 v[66:67], v[56:57]\n\t" \
    "v_mov_b64 v[50:51], v[40:41]\n\t" \
    "v_mov_b64 v[68:69], v[56:57]\n\t" \
    "v_mov_b64 v[52:53], v[40:41]\n\t" \
    "v_mov_b64 v[70:71], v[56:57]\n\t" \
    "v_mov_b64 v[54:55], v[40:41]\n\t" \
    "s_nop 1\n\t" \
    ".p2align 6\n\t" \
    "1:\n\t" \
    "v_mfma_f32_32x32x16_bf16 v[104:119], v[136:139], %[qb0], v[56:71]\n\t" \
    "ds_read_b128 v[144:147], v168 offset:0\n\t" \
    "ds_read_b128 v[148:151], v168 offset:1024\n\t" \
    "v_exp_f32_e32 v72, v72\n\t" \
    "v_exp_f32_e32 v73, v73\n\t" \
    "v_exp_f32_e32 v74, v74\n\t" \
    "v_smfmac_f32_32x32x32_bf16 v[16:31], v[160:163], v[120:127], %[idx]\n\t" \
    "v_exp_f32_e32 v75, v75\n\t" \
    "v_exp_f32_e32 v76, v76\n\t" \
    "v_exp_f32_e32 v77, v77\n\t" \
    "v_mfma_f32_32x32x16_bf16 v[104:119], v[140:143], %[qb1], v[104:119]\n\t" \
    "ds_read_b128 v[152:155], v168 offset:2048\n\t" \
    "ds_read_b128 v[156:159], v168 offset:3072\n\t" \
    "v_exp_f32_e32 v78, v78\n\t" \
    "v_exp_f32_e32 v79, v79\n\t" \
    "v_exp_f32_e32 v80, v80\n\t" \
    "v_smfmac_f32_16x16x64_bf16 v[36:39], %[ones], v[120:127], %[idx]\n\t" \
    "v_exp_f32_e32 v81, v81\n\t" \
    "v_exp_f32_e32 v82, v82\n\t" \
    "v_smfmac_f32_32x32x32_bf16 v[16:31], v[164:167], v[128:135], %[idx]\n\t" \
    "v_exp_f32_e32 v83, v83\n\t" \
    "v_exp_f32_e32 v84, v84\n\t" \
    "v_exp_f32_e32 v85, v85\n\t" \
    "v_smfmac_f32_16x16x64_bf16 v[36:39], %[ones], v[128:135], %[idx]\n\t" \
    "v_exp_f32_e32 v86, v86\n\t" \
    "v_exp_f32_e32 v87, v87\n\t" \
    "s_waitcnt lgkmcnt(0)\n\t" \
    "v_mfma_f32_32x32x16_bf16 v[88:103], v[144:147], %[qa0], v[40:55]\n\t" \
    "v_exp_f32_e32 v104, v104\n\t" \
    "v_exp_f32_e32 v105, v105\n\t" \
    "v_exp_f32_e32 v106, v106\n\t" \
    "v_smfmac_f32_32x32x32_bf16 v[0:15], v[152:155], v[72:79], %[idx]\n\t" \
    "v_exp_f32_e32 v107, v107\n\t" \
    "v_exp_f32_e32 v108, v108\n\t" \
    "v_exp_f32_e32 v109, v109\n\t" \
    "v_mfma_f32_32x32x16_bf16 v[88:103], v[148:151], %[qa1], v[88:103]\n\t" \
    "v_exp_f32_e32 v110, v110\n\t" \
    "v_exp_f32_e32 v111, v111\n\t" \
    "v_exp_f32_e32 v112, v112\n\t" \
    "v_smfmac_f32_16x16x64_bf16 v[32:35], %[ones], v[72:79], %[idx]\n\t" \
    "v_exp_f32_e32 v113, v113\n\t" \
    "v_exp_f32_e32 v114, v114\n\t" \
    "v_smfmac_f32_32x32x32_bf16 v[0:15], v[156:159], v[80:87], %[idx]\n\t" \
    "v_exp_f32_e32 v115, v115\n\t" \
    "v_exp_f32_e32 v116, v116\n\t" \
    "v_exp_f32_e32 v117, v117\n\t" \
    "v_smfmac_f32_16x16x64_bf16 v[32:35], %[ones], v[80:87], %[idx]\n\t" \
    "v_exp_f32_e32 v118, v118\n\t" \
    "v_exp_f32_e32 v119, v119\n\t" \
    "v_mfma_f32_32x32x16_bf16 v[120:135], v[144:147], %[qb0], v[56:71]\n\t" \
    "ds_read_b128 v[136:139], v168 offset:4096\n\t" \
    "ds_read_b128 v[140:143], v168 offset:5120\n\t" \
    "v_exp_f32_e32 v88, v88\n\t" \
    "v_exp_f32_e32 v89, v89\n\t" \
    "v_exp_f32_e32 v90, v90\n\t" \
    "v_smfmac_f32_32x32x32_bf16 v[16:31], v[152:155], v[104:111], %[idx]\n\t" \
    "v_exp_f32_e32 v91, v91\n\t" \
    "v_exp_f32_e32 v92, v92\n\t" \
    "v_exp_f32_e32 v93, v93\n\t" \
    "v_mfma_f32_32x32x16_bf16 v[120:135], v[148:151], %[qb1], v[120:135]\n\t" \
    "ds_read_b128 v[160:163], v168 offset:6144\n\t" \
    "ds_read_b128 v[164:167], v168 offset:7168\n\t" \
    "v_exp_f32_e32 v94, v94\n\t" \
    "v_exp_f32_e32 v95, v95\n\t" \
    "v_exp_f32_e32 v96, v96\n\t" \
    "v_smfmac_f32_16x16x64_bf16 v[36:39], %[ones], v[104:111], %[idx]\n\t" \
    "v_exp_f32_e32 v97, v97\n\t" \
    "v_exp_f32_e32 v98, v98\n\t" \
    "v_smfmac_f32_32x32x32_bf16 v[16:31], v[156:159], v[112:119], %[idx]\n\t" \
    "v_exp_f32_e32 v99, v99\n\t" \
    "v_exp_f32_e32 v100, v100\n\t" \
    "v_exp_f32_e32 v101, v101\n\t" \
    "v_smfmac_f32_16x16x64_bf16 v[36:39], %[ones], v[112:119], %[idx]\n\t" \
    "v_exp_f32_e32 v102, v102\n\t" \
    "v_exp_f32_e32 v103, v103\n\t" \
    "s_waitcnt lgkmcnt(0)\n\t" \
    "v_mfma_f32_32x32x16_bf16 v[72:87], v[136:139], %[qa0], v[40:55]\n\t" \
    "v_exp_f32_e32 v120, v120\n\t" \
    "v_exp_f32_e32 v121, v121\n\t" \
    "v_exp_f32_e32 v122, v122\n\t" \
    "v_smfmac_f32_32x32x32_bf16 v[0:15], v[160:163], v[88:95], %[idx]\n\t" \
    "v_exp_f32_e32 v123, v123\n\t" \
    "v_exp_f32_e32 v124, v124\n\t" \
    "v_exp_f32_e32 v125, v125\n\t" \
    "v_mfma_f32_32x32x16_bf16 v[72:87], v[140:143], %[qa1], v[72:87]\n\t" \
    "v_exp_f32_e32 v126, v126\n\t" \
    "v_exp_f32_e32 v127, v127\n\t" \
    "v_exp_f32_e32 v128, v128\n\t" \
    "v_smfmac_f32_16x16x64_bf16 v[32:35], %[ones], v[88:95], %[idx]\n\t" \
    "v_exp_f32_e32 v129, v129\n\t" \
    "v_exp_f32_e32 v130, v130\n\t" \
    "v_smfmac_f32_32x32x32_bf16 v[0:15], v[164:167], v[96:103], %[idx]\n\t" \
    "v_exp_f32_e32 v131, v131\n\t" \
    "v_exp_f32_e32 v132, v132\n\t" \
    "v_exp_f32_e32 v133, v133\n\t" \
    "v_smfmac_f32_16x16x64_bf16 v[32:35], %[ones], v[96:103], %[idx]\n\t" \
    "v_exp_f32_e32 v134, v134\n\t" \
    "v_exp_f32_e32 v135, v135\n\t" \
    "v_mfma_f32_32x32x16_bf16 v[104:119], v[136:139], %[qb0], v[56:71]\n\t" \
    "ds_read_b128 v[144:147], v168 offset:8192\n\t" \
    "ds_read_b128 v[148:151], v168 offset:9216\n\t" \
    "v_exp_f32_e32 v72, v72\n\t" \
    "v_exp_f32_e32 v73, v73\n\t" \
    "v_exp_f32_e32 v74, v74\n\t" \
    "v_smfmac_f32_32x32x32_bf16 v[16:31], v[160:163], v[120:127], %[idx]\n\t" \
    "v_exp_f32_e32 v75, v75\n\t" \
    "v_exp_f32_e32 v76, v76\n\t" \
    "v_exp_f32_e32 v77, v77\n\t" \
    "v_mfma_f32_32x32x16_bf16 v[104:119], v[140:143], %[qb1], v[104:119]\n\t" \
    "ds_read_b128 v[152:155], v168 offset:10240\n\t" \
    "ds_read_b128 v[156:159], v168 offset:11264\n\t" \
    "v_exp_f32_e32 v78, v78\n\t" \
    "v_exp_f32_e32 v79, v79\n\t" \
    "v_exp_f32_e32 v80, v80\n\t" \
    "v_smfmac_f32_16x16x64_bf16 v[36:39], %[ones], v[120:127], %[idx]\n\t" \
    "v_exp_f32_e32 v81, v81\n\t" \
    "v_exp_f32_e32 v82, v82\n\t" \
    "v_smfmac_f32_32x32x32_bf16 v[16:31], v[164:167], v[128:135], %[idx]\n\t" \
    "v_exp_f32_e32 v83, v83\n\t" \
    "v_exp_f32_e32 v84, v84\n\t" \
    "v_exp_f32_e32 v85, v85\n\t" \
    "v_smfmac_f32_16x16x64_bf16 v[36:39], %[ones], v[128:135], %[idx]\n\t" \
    "v_exp_f32_e32 v86, v86\n\t" \
    "v_exp_f32_e32 v87, v87\n\t" \
    "s_waitcnt lgkmcnt(0)\n\t" \
    "v_mfma_f32_32x32x16_bf16 v[88:103], v[144:147], %[qa0], v[40:55]\n\t" \
    "v_exp_f32_e32 v104, v104\n\t" \
    "v_exp_f32_e32 v105, v105\n\t" \
    "v_exp_f32_e32 v106, v106\n\t" \
    "v_smfmac_f32_32x32x32_bf16 v[0:15], v[152:155], v[72:79], %[idx]\n\t" \
    "v_exp_f32_e32 v107, v107\n\t" \
    "v_exp_f32_e32 v108, v108\n\t" \
    "v_exp_f32_e32 v109, v109\n\t" \
    "v_mfma_f32_32x32x16_bf16 v[88:103], v[148:151], %[qa1], v[88:103]\n\t" \
    "v_exp_f32_e32 v110, v110\n\t" \
    "v_exp_f32_e32 v111, v111\n\t" \
    "v_exp_f32_e32 v112, v112\n\t" \
    "v_smfmac_f32_16x16x64_bf16 v[32:35], %[ones], v[72:79], %[idx]\n\t" \
    "v_exp_f32_e32 v113, v113\n\t" \
    "v_exp_f32_e32 v114, v114\n\t" \
    "v_smfmac_f32_32x32x32_bf16 v[0:15], v[156:159], v[80:87], %[idx]\n\t" \
    "v_exp_f32_e32 v115, v115\n\t" \
    "v_exp_f32_e32 v116, v116\n\t" \
    "v_exp_f32_e32 v117, v117\n\t" \
    "v_smfmac_f32_16x16x64_bf16 v[32:35], %[ones], v[80:87], %[idx]\n\t" \
    "v_exp_f32_e32 v118, v118\n\t" \
    "v_exp_f32_e32 v119, v119\n\t" \
    "v_mfma_f32_32x32x16_bf16 v[120:135], v[144:147], %[qb0], v[56:71]\n\t" \
    "ds_read_b128 v[136:139], v168 offset:12288\n\t" \
    "ds_read_b128 v[140:143], v168 offset:13312\n\t" \
    "v_exp_f32_e32 v88, v88\n\t" \
    "v_exp_f32_e32 v89, v89\n\t" \
    "v_exp_f32_e32 v90, v90\n\t" \
    "v_smfmac_f32_32x32x32_bf16 v[16:31], v[152:155], v[104:111], %[idx]\n\t" \
    "v_exp_f32_e32 v91, v91\n\t" \
    "v_exp_f32_e32 v92, v92\n\t" \
    "v_exp_f32_e32 v93, v93\n\t" \
    "v_mfma_f32_32x32x16_bf16 v[120:135], v[148:151], %[qb1], v[120:135]\n\t" \
    "ds_read_b128 v[160:163], v168 offset:14336\n\t" \
    "ds_read_b128 v[164:167], v168 offset:15360\n\t" \
    "v_exp_f32_e32 v94, v94\n\t" \
    "v_exp_f32_e32 v95, v95\n\t" \
    "v_exp_f32_e32 v96, v96\n\t" \
    "v_smfmac_f32_16x16x64_bf16 v[36:39], %[ones], v[104:111], %[idx]\n\t" \
    "v_exp_f32_e32 v97, v97\n\t" \
    "v_exp_f32_e32 v98, v98\n\t" \
    "v_smfmac_f32_32x32x32_bf16 v[16:31], v[156:159], v[112:119], %[idx]\n\t" \
    "v_exp_f32_e32 v99, v99\n\t" \
    "v_exp_f32_e32 v100, v100\n\t" \
    "v_exp_f32_e32 v101, v101\n\t" \
    "v_smfmac_f32_16x16x64_bf16 v[36:39], %[ones], v[112:119], %[idx]\n\t" \
    "v_exp_f32_e32 v102, v102\n\t" \
    "v_exp_f32_e32 v103, v103\n\t" \
    "s_waitcnt lgkmcnt(0)\n\t" \
    "v_mfma_f32_32x32x16_bf16 v[72:87], v[136:139], %[qa0], v[40:55]\n\t" \
    "v_exp_f32_e32 v120, v120\n\t" \
    "v_exp_f32_e32 v121, v121\n\t" \
    "v_exp_f32_e32 v122, v122\n\t" \
    "v_smfmac_f32_32x32x32_bf16 v[0:15], v[160:163], v[88:95], %[idx]\n\t" \
    "v_exp_f32_e32 v123, v123\n\t" \
    "v_exp_f32_e32 v124, v124\n\t" \
    "v_exp_f32_e32 v125, v125\n\t" \
    "v_mfma_f32_32x32x16_bf16 v[72:87], v[140:143], %[qa1], v[72:87]\n\t" \
    "v_exp_f32_e32 v126, v126\n\t" \
    "v_exp_f32_e32 v127, v127\n\t" \
    "v_exp_f32_e32 v128, v128\n\t" \
    "v_smfmac_f32_16x16x64_bf16 v[32:35], %[ones], v[88:95], %[idx]\n\t" \
    "v_exp_f32_e32 v129, v129\n\t" \
    "v_exp_f32_e32 v130, v130\n\t" \
    "v_smfmac_f32_32x32x32_bf16 v[0:15], v[164:167], v[96:103], %[idx]\n\t" \
    "v_exp_f32_e32 v131, v131\n\t" \
    "v_exp_f32_e32 v132, v132\n\t" \
    "v_exp_f32_e32 v133, v133\n\t" \
    "v_smfmac_f32_16x16x64_bf16 v[32:35], %[ones], v[96:103], %[idx]\n\t" \
    "v_exp_f32_e32 v134, v134\n\t" \
    "v_exp_f32_e32 v135, v135\n\t" \
    "v_mfma_f32_32x32x16_bf16 v[104:119], v[136:139], %[qb0], v[56:71]\n\t" \
    "ds_read_b128 v[144:147], v168 offset:16384\n\t" \
    "ds_read_b128 v[148:151], v168 offset:17408\n\t" \
    "v_exp_f32_e32 v72, v72\n\t" \
    "v_exp_f32_e32 v73, v73\n\t" \
    "v_exp_f32_e32 v74, v74\n\t" \
    "v_smfmac_f32_32x32x32_bf16 v[16:31], v[160:163], v[120:127], %[idx]\n\t" \
    "v_exp_f32_e32 v75, v75\n\t" \
    "v_exp_f32_e32 v76, v76\n\t" \
    "v_exp_f32_e32 v77, v77\n\t" \
    "v_mfma_f32_32x32x16_bf16 v[104:119], v[140:143], %[qb1], v[104:119]\n\t" \
    "ds_read_b128 v[152:155], v168 offset:18432\n\t" \
    "ds_read_b128 v[156:159], v168 offset:19456\n\t" \
    "v_exp_f32_e32 v78, v78\n\t" \
    "v_exp_f32_e32 v79, v79\n\t" \
    "v_exp_f32_e32 v80, v80\n\t" \
    "v_smfmac_f32_16x16x64_bf16 v[36:39], %[ones], v[120:127], %[idx]\n\t" \
    "v_exp_f32_e32 v81, v81\n\t" \
    "v_exp_f32_e32 v82, v82\n\t" \
    "v_smfmac_f32_32x32x32_bf16 v[16:31], v[164:167], v[128:135], %[idx]\n\t" \
    "v_exp_f32_e32 v83, v83\n\t" \
    "v_exp_f32_e32 v84, v84\n\t" \
    "v_exp_f32_e32 v85, v85\n\t" \
    "v_smfmac_f32_16x16x64_bf16 v[36:39], %[ones], v[128:135], %[idx]\n\t" \
    "v_exp_f32_e32 v86, v86\n\t" \
    "v_exp_f32_e32 v87, v87\n\t" \
    "s_waitcnt lgkmcnt(0)\n\t" \
    "v_mfma_f32_32x32x16_bf16 v[88:103], v[144:147], %[qa0], v[40:55]\n\t" \
    "v_exp_f32_e32 v104, v104\n\t" \
    "v_exp_f32_e32 v105, v105\n\t" \
    "v_exp_f32_e32 v106, v106\n\t" \
    "v_smfmac_f32_32x32x32_bf16 v[0:15], v[152:155], v[72:79], %[idx]\n\t" \
    "v_exp_f32_e32 v107, v107\n\t" \
    "v_exp_f32_e32 v108, v108\n\t" \
    "v_exp_f32_e32 v109, v109\n\t" \
    "v_mfma_f32_32x32x16_bf16 v[88:103], v[148:151], %[qa1], v[88:103]\n\t" \
    "v_exp_f32_e32 v110, v110\n\t" \
    "v_exp_f32_e32 v111, v111\n\t" \
    "v_exp_f32_e32 v112, v112\n\t" \
    "v_smfmac_f32_16x16x64_bf16 v[32:35], %[ones], v[72:79], %[idx]\n\t" \
    "v_exp_f32_e32 v113, v113\n\t" \
    "v_exp_f32_e32 v114, v114\n\t" \
    "v_smfmac_f32_32x32x32_bf16 v[0:15], v[156:159], v[80:87], %[idx]\n\t" \
    "v_exp_f32_e32 v115, v115\n\t" \
    "v_exp_f32_e32 v116, v116\n\t" \
    "v_exp_f32_e32 v117, v117\n\t" \
    "v_smfmac_f32_16x16x64_bf16 v[32:35], %[ones], v[80:87], %[idx]\n\t" \
    "v_exp_f32_e32 v118, v118\n\t" \
    "v_exp_f32_e32 v119, v119\n\t" \
    "v_mfma_f32_32x32x16_bf16 v[120:135], v[144:147], %[qb0], v[56:71]\n\t" \
    "ds_read_b128 v[136:139], v168 offset:20480\n\t" \
    "ds_read_b128 v[140:143], v168 offset:21504\n\t" \
    "v_exp_f32_e32 v88, v88\n\t" \
    "v_exp_f32_e32 v89, v89\n\t" \
    "v_exp_f32_e32 v90, v90\n\t" \
    "v_smfmac_f32_32x32x32_bf16 v[16:31], v[152:155], v[104:111], %[idx]\n\t" \
    "v_exp_f32_e32 v91, v91\n\t" \
    "v_exp_f32_e32 v92, v92\n\t" \
    "v_exp_f32_e32 v93, v93\n\t" \
    "v_mfma_f32_32x32x16_bf16 v[120:135], v[148:151], %[qb1], v[120:135]\n\t" \
    "ds_read_b128 v[160:163], v168 offset:22528\n\t" \
    "ds_read_b128 v[164:167], v168 offset:23552\n\t" \
    "v_exp_f32_e32 v94, v94\n\t" \
    "v_exp_f32_e32 v95, v95\n\t" \
    "v_exp_f32_e32 v96, v96\n\t" \
    "v_smfmac_f32_16x16x64_bf16 v[36:39], %[ones], v[104:111], %[idx]\n\t" \
    "v_exp_f32_e32 v97, v97\n\t" \
    "v_exp_f32_e32 v98, v98\n\t" \
    "v_smfmac_f32_32x32x32_bf16 v[16:31], v[156:159], v[112:119], %[idx]\n\t" \
    "v_exp_f32_e32 v99, v99\n\t" \
    "v_exp_f32_e32 v100, v100\n\t" \
    "v_exp_f32_e32 v101, v101\n\t" \
    "v_smfmac_f32_16x16x64_bf16 v[36:39], %[ones], v[112:119], %[idx]\n\t" \
    "v_exp_f32_e32 v102, v102\n\t" \
    "v_exp_f32_e32 v103, v103\n\t" \
    "s_waitcnt lgkmcnt(0)\n\t" \
    "v_mfma_f32_32x32x16_bf16 v[72:87], v[136:139], %[qa0], v[40:55]\n\t" \
    "v_exp_f32_e32 v120, v120\n\t" \
    "v_exp_f32_e32 v121, v121\n\t" \
    "v_exp_f32_e32 v122, v122\n\t" \
    "v_smfmac_f32_32x32x32_bf16 v[0:15], v[160:163], v[88:95], %[idx]\n\t" \
    "v_exp_f32_e32 v123, v123\n\t" \
    "v_exp_f32_e32 v124, v124\n\t" \
    "v_exp_f32_e32 v125, v125\n\t" \
    "v_mfma_f32_32x32x16_bf16 v[72:87], v[140:143], %[qa1], v[72:87]\n\t" \
    "v_exp_f32_e32 v126, v126\n\t" \
    "v_exp_f32_e32 v127, v127\n\t" \
    "v_exp_f32_e32 v128, v128\n\t" \
    "v_smfmac_f32_16x16x64_bf16 v[32:35], %[ones], v[88:95], %[idx]\n\t" \
    "v_exp_f32_e32 v129, v129\n\t" \
    "v_exp_f32_e32 v130, v130\n\t" \
    "v_smfmac_f32_32x32x32_bf16 v[0:15], v[164:167], v[96:103], %[idx]\n\t" \
    "v_exp_f32_e32 v131, v131\n\t" \
    "v_exp_f32_e32 v132, v132\n\t" \
    "v_exp_f32_e32 v133, v133\n\t" \
    "v_smfmac_f32_16x16x64_bf16 v[32:35], %[ones], v[96:103], %[idx]\n\t" \
    "v_exp_f32_e32 v134, v134\n\t" \
    "v_exp_f32_e32 v135, v135\n\t" \
    "v_mfma_f32_32x32x16_bf16 v[104:119], v[136:139], %[qb0], v[56:71]\n\t" \
    "ds_read_b128 v[144:147], v168 offset:24576\n\t" \
    "ds_read_b128 v[148:151], v168 offset:25600\n\t" \
    "v_exp_f32_e32 v72, v72\n\t" \
    "v_exp_f32_e32 v73, v73\n\t" \
    "v_exp_f32_e32 v74, v74\n\t" \
    "v_smfmac_f32_32x32x32_bf16 v[16:31], v[160:163], v[120:127], %[idx]\n\t" \
    "v_exp_f32_e32 v75, v75\n\t" \
    "v_exp_f32_e32 v76, v76\n\t" \
    "v_exp_f32_e32 v77, v77\n\t" \
    "v_mfma_f32_32x32x16_bf16 v[104:119], v[140:143], %[qb1], v[104:119]\n\t" \
    "ds_read_b128 v[152:155], v168 offset:26624\n\t" \
    "ds_read_b128 v[156:159], v168 offset:27648\n\t" \
    "v_exp_f32_e32 v78, v78\n\t" \
    "v_exp_f32_e32 v79, v79\n\t" \
    "v_exp_f32_e32 v80, v80\n\t" \
    "v_smfmac_f32_16x16x64_bf16 v[36:39], %[ones], v[120:127], %[idx]\n\t" \
    "v_exp_f32_e32 v81, v81\n\t" \
    "v_exp_f32_e32 v82, v82\n\t" \
    "v_smfmac_f32_32x32x32_bf16 v[16:31], v[164:167], v[128:135], %[idx]\n\t" \
    "v_exp_f32_e32 v83, v83\n\t" \
    "v_exp_f32_e32 v84, v84\n\t" \
    "v_exp_f32_e32 v85, v85\n\t" \
    "v_smfmac_f32_16x16x64_bf16 v[36:39], %[ones], v[128:135], %[idx]\n\t" \
    "v_exp_f32_e32 v86, v86\n\t" \
    "v_exp_f32_e32 v87, v87\n\t" \
    "s_waitcnt lgkmcnt(0)\n\t" \
    "v_mfma_f32_32x32x16_bf16 v[88:103], v[144:147], %[qa0], v[40:55]\n\t" \
    "v_exp_f32_e32 v104, v104\n\t" \
    "v_exp_f32_e32 v105, v105\n\t" \
    "v_exp_f32_e32 v106, v106\n\t" \
    "v_smfmac_f32_32x32x32_bf16 v[0:15], v[152:155], v[72:79], %[idx]\n\t" \
    "v_exp_f32_e32 v107, v107\n\t" \
    "v_exp_f32_e32 v108, v108\n\t" \
    "v_exp_f32_e32 v109, v109\n\t" \
    "v_mfma_f32_32x32x16_bf16 v[88:103], v[148:151], %[qa1], v[88:103]\n\t" \
    "v_exp_f32_e32 v110, v110\n\t" \
    "v_exp_f32_e32 v111, v111\n\t" \
    "v_exp_f32_e32 v112, v112\n\t" \
    "v_smfmac_f32_16x16x64_bf16 v[32:35], %[ones], v[72:79], %[idx]\n\t" \
    "v_exp_f32_e32 v113, v113\n\t" \
    "v_exp_f32_e32 v114, v114\n\t" \
    "v_smfmac_f32_32x32x32_bf16 v[0:15], v[156:159], v[80:87], %[idx]\n\t" \
    "v_exp_f32_e32 v115, v115\n\t" \
    "v_exp_f32_e32 v116, v116\n\t" \
    "v_exp_f32_e32 v117, v117\n\t" \
    "v_smfmac_f32_16x16x64_bf16 v[32:35], %[ones], v[80:87], %[idx]\n\t" \
    "v_exp_f32_e32 v118, v118\n\t" \
    "v_exp_f32_e32 v119, v119\n\t" \
    "v_mfma_f32_32x32x16_bf16 v[120:135], v[144:147], %[qb0], v[56:71]\n\t" \
    "ds_read_b128 v[136:139], v168 offset:28672\n\t" \
    "ds_read_b128 v[140:143], v168 offset:29696\n\t" \
    "v_exp_f32_e32 v88, v88\n\t" \
    "v_exp_f32_e32 v89, v89\n\t" \
    "v_exp_f32_e32 v90, v90\n\t" \
    "v_smfmac_f32_32x32x32_bf16 v[16:31], v[152:155], v[104:111], %[idx]\n\t" \
    "v_exp_f32_e32 v91, v91\n\t" \
    "v_exp_f32_e32 v92, v92\n\t" \
    "v_exp_f32_e32 v93, v93\n\t" \
    "v_mfma_f32_32x32x16_bf16 v[120:135], v[148:151], %[qb1], v[120:135]\n\t" \
    "ds_read_b128 v[160:163], v168 offset:30720\n\t" \
    "ds_read_b128 v[164:167], v168 offset:31744\n\t" \
    "v_exp_f32_e32 v94, v94\n\t" \
    "v_exp_f32_e32 v95, v95\n\t" \
    "v_exp_f32_e32 v96, v96\n\t" \
    "v_smfmac_f32_16x16x64_bf16 v[36:39], %[ones], v[104:111], %[idx]\n\t" \
    "v_exp_f32_e32 v97, v97\n\t" \
    "v_exp_f32_e32 v98, v98\n\t" \
    "v_smfmac_f32_32x32x32_bf16 v[16:31], v[156:159], v[112:119], %[idx]\n\t" \
    "v_exp_f32_e32 v99, v99\n\t" \
    "v_exp_f32_e32 v100, v100\n\t" \
    "v_exp_f32_e32 v101, v101\n\t" \
    "v_smfmac_f32_16x16x64_bf16 v[36:39], %[ones], v[112:119], %[idx]\n\t" \
    "v_exp_f32_e32 v102, v102\n\t" \
    "v_exp_f32_e32 v103, v103\n\t" \
    "s_waitcnt lgkmcnt(0)\n\t" \
    "v_mfma_f32_32x32x16_bf16 v[72:87], v[136:139], %[qa0], v[40:55]\n\t" \
    "v_exp_f32_e32 v120, v120\n\t" \
    "v_exp_f32_e32 v121, v121\n\t" \
    "v_exp_f32_e32 v122, v122\n\t" \
    "v_smfmac_f32_32x32x32_bf16 v[0:15], v[160:163], v[88:95], %[idx]\n\t" \
    "v_exp_f32_e32 v123, v123\n\t" \
    "v_exp_f32_e32 v124, v124\n\t" \
    "v_exp_f32_e32 v125, v125\n\t" \
    "v_mfma_f32_32x32x16_bf16 v[72:87], v[140:143], %[qa1], v[72:87]\n\t" \
    "v_exp_f32_e32 v126, v126\n\t" \
    "v_exp_f32_e32 v127, v127\n\t" \
    "v_exp_f32_e32 v128, v128\n\t" \
    "v_smfmac_f32_16x16x64_bf16 v[32:35], %[ones], v[88:95], %[idx]\n\t" \
    "v_exp_f32_e32 v129, v129\n\t" \
    "v_exp_f32_e32 v130, v130\n\t" \
    "v_smfmac_f32_32x32x32_bf16 v[0:15], v[164:167], v[96:103], %[idx]\n\t" \
    "v_exp_f32_e32 v131, v131\n\t" \
    "v_exp_f32_e32 v132, v132\n\t" \
    "v_exp_f32_e32 v133, v133\n\t" \
    "v_smfmac_f32_16x16x64_bf16 v[32:35], %[ones], v[96:103], %[idx]\n\t" \
    "v_exp_f32_e32 v134, v134\n\t" \
    "v_exp_f32_e32 v135, v135\n\t" \
    "s_add_u32 %[cnt], %[cnt], 2\n\t" \
    "s_lshl_b32 %[tmp], %[cnt], 14\n\t" \
    "s_and_b32 %[tmp], %[tmp], 0x1ffff\n\t" \
    "v_add_u32 v168, %[tmp], %[lad]\n\t" \
    "s_bitcmp1_b32 0x1110, %[cnt]\n\t" \
    "s_cbranch_scc0 2f\n\t" \
    "s_barrier\n\t" \
    "2:\n\t" \
    "s_cmp_lt_u32 %[cnt], 16\n\t" \
    "s_cbranch_scc1 1b\n\t" \
    "s_nop 1\n\t" \
    "v_smfmac_f32_32x32x32_bf16 v[16:31], v[160:163], v[120:127], %[idx]\n\t" \
    "v_smfmac_f32_16x16x64_bf16 v[36:39], %[ones], v[120:127], %[idx]\n\t" \
    "v_smfmac_f32_32x32x32_bf16 v[16:31], v[164:167], v[128:135], %[idx]\n\t" \
    "v_smfmac_f32_16x16x64_bf16 v[36:39], %[ones], v[128:135], %[idx]\n\t" \
    "s_nop 7\n\t" \
    "s_nop 7\n\t" \
    "s_nop 7\n\t"
#define ATTN_CLOB "v40", "v41", "v42", "v43", "v44", "v45", "v46", "v47", "v48", "v49", "v50", "v51", "v52", "v53", "v54", "v55", "v56", "v57", "v58", "v59", "v60", "v61", "v62", "v63", "v64", "v65", "v66", "v67", "v68", "v69", "v70", "v71", "v72", "v73", "v74", "v75", "v76", "v77", "v78", "v79", "v80", "v81", "v82", "v83", "v84", "v85", "v86", "v87", "v88", "v89", "v90", "v91", "v92", "v93", "v94", "v95", "v96", "v97", "v98", "v99", "v100", "v101", "v102", "v103", "v104", "v105", "v106", "v107", "v108", "v109", "v110", "v111", "v112", "v113", "v114", "v115", "v116", "v117", "v118", "v119", "v120", "v121", "v122", "v123", "v124", "v125", "v126", "v127", "v128", "v129", "v130", "v131", "v132", "v133", "v134", "v135", "v136", "v137", "v138", "v139", "v140", "v141", "v142", "v143", "v144", "v145", "v146", "v147", "v148", "v149", "v150", "v151", "v152", "v153", "v154", "v155", "v156", "v157", "v158", "v159", "v160", "v161", "v162", "v163", "v164", "v165", "v166", "v167", "v168"

DI float rowmax16(const f32x16& s) {
  float m = fmaxf(fmaxf(s[0], s[1]), s[2]);
#pragma unroll
  for (int i = 3; i < 15; i += 2) m = fmaxf(fmaxf(m, s[i]), s[i + 1]);
  m = fmaxf(m, s[15]);
  auto rr = __builtin_amdgcn_permlane32_swap(__float_as_uint(m), __float_as_uint(m), false, false);
  return fmaxf(__uint_as_float(rr[0]), __uint_as_float(rr[1]));
}
DI f32x16 attn_exact(srd_t sws, u32x4 q0, u32x4 q1, int lane) {
  f32x16 o = {}; float m = -INFINITY, l = 0.f;
  for (int t = 0; t < 64; ++t) {
    const unsigned kp = WS_KVB + ((t + 63) & 63) * 4096 + lane * 16, vp = WS_KVB + t * 4096 + 2048 + lane * 16;
    f32x16 s = MFMA32(BC8(ld16(sws, kp)), BC8(q0), f32x16{}); s = MFMA32(BC8(ld16(sws, kp + 1024)), BC8(q1), s);
    const float mn = fmaxf(m, rowmax16(s)), al = __builtin_amdgcn_exp2f(m - mn);
    float ps = 0.f;
#pragma unroll
    for (int i = 0; i < 16; ++i) { s[i] = __uint_as_float(__float_as_uint(__builtin_amdgcn_exp2f(s[i] - mn)) & 0xffff0000u); ps += s[i]; }
    ps += __shfl_xor(ps, 32);
    l = l * al + ps; m = mn; o = o * al;
    const u32x4 v0 = ld16(sws, vp), v1 = ld16(sws, vp + 1024);
    const f32x8 s0 = __builtin_shufflevector(s, s, 0, 1, 2, 3, 4, 5, 6, 7), s1 = __builtin_shufflevector(s, s, 8, 9, 10, 11, 12, 13, 14, 15);
    asm volatile("s_nop 1\n\tv_smfmac_f32_32x32x32_bf16 %0, %1, %2, %5\n\tv_smfmac_f32_32x32x32_bf16 %0, %3, %4, %5\n\ts_nop 7\n\ts_nop 7\n\ts_nop 7"
                 : "+v"(o) : "v"(v0), "v"(s0), "v"(v1), "v"(s1), "v"(0xddddddddu));
  }
  return o * (1.0f / l);
}
DI void attn_finish(f32x16 o, const f32x4& l, u32x4 q0, u32x4 q1, int lane, srd_t sws, unsigned dst) {
  const float ls = (lane & 16) ? l[1] : l[0];
  o = o * __builtin_amdgcn_rcpf(ls);
  float chk = fabsf(o[0]);
#pragma unroll
  for (int i = 1; i < 15; i += 2) asm("v_maximum3_f32 %0, %0, |%1|, |%2|" : "+v"(chk) : "v"(o[i]), "v"(o[i + 1]));
  const bool bad = !(ls < 1.2676506e30f) || !(chk < 3e38f) || !(fabsf(o[15]) < 3e38f);
  if (__builtin_expect(__any(bad), 0)) o = attn_exact(sws, q0, q1, lane);
  st16(sws, dst, pack8(o, 0)); st16(sws, dst + 1024, pack8(o, 8));
}
__global__ __launch_bounds__(512, 2) void fused(const char* __restrict__ xA, const char* __restrict__ wA, const char* __restrict__ woA, const char* __restrict__ xB,
                                                const char* __restrict__ wqB, const char* __restrict__ wkB, const char* __restrict__ wvB, const char* __restrict__ woB,
                                                char* __restrict__ out, char* __restrict__ ws) {
  extern __shared__ __attribute__((aligned(16))) char smem[];
  lds_ptr lds = (lds_ptr)smem;
  const unsigned lds0 = (unsigned)(uintptr_t)smem;
  const int lane = threadIdx.x & 63, wid = rfl(threadIdx.x >> 6);
  const int xcd = blockIdx.x & 7, idx = blockIdx.x >> 3;
  const int j = (xcd >> 1) * 8 + (idx & 7), hg = (xcd & 1) * 4 + (idx >> 3);
  const int rt = (xcd & 1) * 8 + (idx & 7), hp = (xcd >> 1) * 4 + (idx >> 3);
  const int br = idx & 1, rq = (idx >> 1) & 3, ro = (xcd & 3) * 4 + rq, lc = (xcd >> 2) * 4 + (idx >> 3);
  u64 tag, epoch;
  asm volatile("s_load_dwordx2 %0, %1, 0x0" : "=s"(epoch) : "s"(ws), "s"(xA), "s"(wA), "s"(xB), "s"(wqB), "s"(wkB), "s"(wvB), "s"(woA), "s"(woB), "s"(out) : "memory");
#define GET_TAG() do { asm volatile("s_waitcnt lgkmcnt(0)\n\ts_mov_b64 %0, %1" : "=s"(tag) : "s"(epoch) : "memory"); tag += 1; } while (0)
  const srd_t sws = mk_srd(ws, WS_BYTES);
  const int lw = wid - 4;
  const unsigned vx0 = (lane >> 3) * 2048 + (((lane & 7) ^ (lane >> 4)) << 4), vx1 = vx0 ^ 64;
  const unsigned cw = (lane & 7) ^ (((lane >> 4) & 1) << 1);
  const srd_t sxb = mk_srd(xB, 4u << 20), swb = mk_srd(wqB, 2u << 20), seb = mk_srd((hg & 1) ? wvB : wkB, 65536);
  const unsigned vb0 = (lane >> 3) * 2048 + (cw << 4), vb1 = vb0 ^ 64, ve = (lane >> 2) * 64 + (((lane & 3) ^ ((lane >> 5) << 1)) << 4);
  const unsigned oxb = (j * 64 + lw * 16) * 2048, owb = (lw & 1) * 32 * 2048 + hg * 256 + (lw >> 1) * 128;
  const unsigned k0b = ((8 * ((idx + (idx >> 3)) & 1) + 4 * (idx >> 4) + 2 * ((xcd >> 1) + (xcd & 1))) & 15);
  auto issue_b = [&](unsigned ks, unsigned slot) {
    const unsigned d = lds0 + slot, kt = (ks + k0b) & 15;
    dma16(d + lw * 2048, vx0, sxb, oxb + kt * 128); dma16(d + lw * 2048 + 1024, vx1, sxb, oxb + kt * 128 + 16384);
#pragma unroll
    for (int i = 0; i < 4; ++i) dma16(d + P1_W + lw * 4096 + i * 1024, (i & 1) ? vb1 : vb0, swb, owb + (kt * 64 + i * 8) * 2048);
    dma16(d + P1_W + 16384 + lw * 1024, ve, seb, kt * 4096 + lw * 1024);
  };
  if (wid >= 4) { issue_b(0, 0); issue_b(1, P1_SLOT); issue_b(2, 2 * P1_SLOT); }
  if (wid >= 4) {
    __builtin_amdgcn_s_setprio(3);
    {
      const srd_t sxa = mk_srd(xA, 4u << 20), swa = mk_srd(wA, 6u << 20);
      const unsigned va0 = (lane >> 3) * 6144 + (cw << 4), va1 = va0 ^ 64;
      const unsigned oxa = (rt * 128 + lw * 32) * 2048, owa = lw * 16 * 6144 + hp * 128;
      const unsigned k0a = ((4 * ((idx & 3) + (xcd >> 1)) + 2 * (xcd & 1)) & 15);
      auto issue_a = [&](unsigned ks, unsigned slot) {
        const unsigned d = lds0 + slot, kt = (ks + k0a) & 15;
#pragma unroll
        for (int i = 0; i < 4; ++i) dma16(d + lw * 4096 + i * 1024, (i & 1) ? vx1 : vx0, sxa, oxa + kt * 128 + i * 16384);
#pragma unroll
        for (int i = 0; i < 6; ++i) dma16(d + P1_W + (i >> 1) * 8192 + lw * 2048 + (i & 1) * 1024, (i & 1) ? va1 : va0, swa, owa + (kt * 64 + (i & 1) * 8) * 6144 + (i >> 1) * 2048);
      };
      GET_TAG();
      WAITV(14); BARRIER();
      unsigned slot = 0;
#define NEXT_SLOT() slot = (slot == 2 * P1_SLOT) ? 0 : slot + P1_SLOT
      for (int s = 1; s < 14; ++s) { VBAR(7); issue_b(s + 2, slot); NEXT_SLOT(); }
      VBAR(7); issue_a(0, slot); NEXT_SLOT();
      for (int s = 15; s < 28; ++s) { VBAR(10); issue_a(s - 14, slot); NEXT_SLOT(); }
      u32x4 f0, f1;
      VBAR(10);
      if (lw == 0) asm volatile("global_load_dwordx4 %0, %2, %3 offset:0 sc1\n\tglobal_load_dwordx4 %1, %2, %3 offset:1024 sc1" : "=&v"(f0), "=&v"(f1) : "v"(lane * 16), "s"(ws + WS_F1) : "memory");
      issue_a(14, slot); NEXT_SLOT();
      asm volatile("s_waitcnt vmcnt(10)\n\ts_barrier" : "+v"(f0), "+v"(f1)::"memory");
      if (lw == 0) {
        const unsigned tl = (unsigned)tag, th = (unsigned)(tag >> 32);
        if (!__all(f0[0] == tl && f0[1] == th && f0[2] == tl && f0[3] == th && f1[0] == tl && f1[1] == th && f1[2] == tl && f1[3] == th)) {
#pragma unroll 1
          for (int i = 0; i < 4; ++i) wait_flags(ws, WS_F1 + i * 512 + lane * 8, tag, SPIN_CAP);
        }
        if (blockIdx.x == 0) set_flag(ws, WS_E, tag);
      }
      issue_a(15, slot);
      VBAR(10); VBAR(0); VBAR(0);
    }
    const unsigned vl = lane * 16;
    auto issue_kv = [&](int st) {
#pragma unroll
      for (int i = 0; i < 4; ++i) dma16c(lds0 + (st & 7) * 16384 + lw * 4096 + i * 1024, vl, sws, WS_KVB + st * 16384 + lw * 4096 + i * 1024);
    };
    if (lw < 2) dma16c(lds0 + L_K0 + lw * 1024, vl, sws, WS_KVB + 63 * 4096 + lw * 1024);
    issue_kv(0); issue_kv(1); issue_kv(2); issue_kv(3);
    WAITV(0); BARRIER();
    const int wr = lw >> 1, wc = lw & 1, h = lane >> 5, head = hp * 2 + wc;
    {
      const LAS f32x4* ex = (const LAS f32x4*)(lds + L_XCH + lw * 4096) + lane;
      const LAS f32x4* ez = (const LAS f32x4*)(lds + L_XZ + wr * 256 + wc * 128) + (lane & 3);
      const unsigned ko = WS_KVP + (head * 16 + rt) * 4096 + wr * 2048 + lane * 16;
      st16(sws, ko, __builtin_bit_cast(u32x4, add4(ex[0], ex[128]))); st16(sws, ko + 1024, __builtin_bit_cast(u32x4, add4(ex[64], ex[192])));
      if (lane < 4) st16(sws, WS_ZP + (head * 16 + rt) * 128 + lane * 32 + wr * 16, __builtin_bit_cast(u32x4, add4(ez[0], ez[4])));
      WAITV(0);
      set_flag(ws, WS_F2 + ((head * 16 + rt) * 2 + wr) * 128, tag);
    }
    issue_kv(4); issue_kv(5); issue_kv(6); issue_kv(7);
    WAITV(0); BARRIER();
    {
      wait_flags(ws, WS_F2 + (head * 32 + (lane & 31)) * 128, tag, SPIN_CAP);
#define LDF(o, so) __builtin_bit_cast(f32x4, __builtin_amdgcn_raw_buffer_load_b128(sws, o, so, 16))
      if (wr == 0) {
        const int L = (rt & 7) * 8 + (lane & 7), b = (lane >> 3) & 1;
        const unsigned kp = WS_KVP + head * 65536 + (lane >> 4) * 16384 + ((L >> 4) & 1) * 2048 + b * 1024 + ((L & 15) + 16 * (2 * (rt >> 3) + (L >> 5))) * 16;
        f32x4 a = add4(add4(LDF(kp, 0), LDF(kp, 4096)), add4(LDF(kp, 8192), LDF(kp, 12288)));
        asm volatile("s_nop 1" : "+v"(a));
        const u32x2 r = {cvtpk(xsum2(a[0]), xsum2(a[1])), cvtpk(xsum2(a[2]), xsum2(a[3]))};
        if (lane < 16) st8(sws, WS_KVF + head * 2048 + (rt >> 3) * 1024 + L * 16 + b * 8, r);
        WAITV(0);
        set_flag(ws, WS_F5 + (head * 16 + rt) * 128, tag);
      } else {
        const unsigned zp = WS_ZP + head * 2048 + lane * 16;
        f32x4 z = add4(LDF(zp, 0), LDF(zp, 1024));
        asm volatile("s_nop 1" : "+v"(z));
        const u32x2 zs = {cvtpk(xsum(z[0]), xsum(z[1])), cvtpk(xsum(z[2]), xsum(z[3]))};
        if (lane < 8) *(LAS u32x2*)(lds + L_EXZ + wc * 64 + lane * 8) = zs;
      }
    }
    issue_kv(8); issue_kv(9); issue_kv(10); issue_kv(11);
    WAITV(0); BARRIER();
    {
      wait_flags(ws, WS_F5 + (head * 16 + (lane & 15)) * 128, tag, SPIN_CAP);
      lds_ptr ez = lds + L_EXZ + wc * 64 + h * 16, pq = lds + L_PQ + lw * 4096 + lane * 16;
      const bf16x8 kv0 = BC8(ld16(sws, WS_KVF + head * 2048 + lane * 16)), kv1 = BC8(ld16(sws, WS_KVF + head * 2048 + 1024 + lane * 16)), z0 = lds128(ez), z1 = lds128(ez + 32);
#pragma unroll
      for (int t = 0; t < 2; ++t) {
        const bf16x8 p0 = lds128(pq + t * 2048), p1 = lds128(pq + t * 2048 + 1024);
        f32x16 num = MFMA32(kv0, p0, f32x16{}); num = MFMA32(kv1, p1, num);
        f32x16 den = MFMA32(z0, p0, f32x16{}); den = MFMA32(z1, p1, den);
        float rd = __builtin_amdgcn_rcpf(den[0] + 1e-6f);
        asm volatile("s_nop 7\n\ts_nop 7" : "+v"(num), "+v"(rd));
#pragma unroll
        for (int i = 0; i < 16; ++i) num[i] = muls(num[i], rd);
        const unsigned dst = WS_OA + ((rt * 4 + wr * 2 + t) * 32 + head) * 2048 + lane * 16;
        st16(sws, dst, pack8(num, 0)); st16(sws, dst + 1024, pack8(num, 8));
          __builtin_amdgcn_sched_barrier(0);
      }
    }
    issue_kv(12); issue_kv(13); issue_kv(14); issue_kv(15);
    const srd_t swo = mk_srd(br ? woB : woA, 2u << 20);
    unsigned dummy = 0;
#pragma unroll
    for (int i = 0; i < 2; ++i)
      asm volatile("buffer_load_dword %0, %1, %2, %3 offen" : "+v"(dummy) : "v"((lane >> 1) * 2048 + (lane & 1) * 128), "s"(swo), "s"(((rq * 4 + lw) * 2 + i) * 65536 + lc * 256) : "memory");
    asm volatile("s_waitcnt vmcnt(0)" : "+v"(dummy)::"memory");
    BARRIER();
    if (lw == 0) set_flag(ws, WS_F3 + (rt * 16 + hp) * 128, tag);
    const unsigned vw = (lane >> 3) * 2048 + (((lane & 7) ^ (((lane >> 4) & 3) << 1)) << 4);
    const unsigned oa = (br ? WS_OB : WS_OA) + (ro * 4 + lw) * 65536, ow = (lw & 1) * 32 * 2048 + lc * 256 + (lw >> 1) * 128;
    auto issue_o = [&](unsigned st) {
#pragma unroll
      for (int i = 0; i < 4; ++i) dma16c(lds0 + (st & 3) * 32768 + lw * 4096 + i * 1024, vl, sws, oa + st * 4096 + i * 1024);
    };
    auto issue_w = [&](unsigned st) {
#pragma unroll
      for (int i = 0; i < 4; ++i) dma16(lds0 + (st & 3) * 32768 + 16384 + lw * 4096 + i * 1024, vw, swo, ow + (st * 64 + i * 8) * 2048);
    };
    const unsigned fo = (br ? WS_F4 : WS_F3) + (ro * 16 + (lane & 15)) * 128;
    issue_w(0); issue_w(1);
    const bool early = br == 0 && wait_flags(ws, fo, tag, 24);
    if (early) { issue_o(0); issue_o(1); }
    BARRIER();
    if (lw == 0) set_flag(ws, WS_F4 + (j * 8 + hg) * 128, tag);
    if (early) {
      VBAR(1); issue_w(2); issue_o(2); issue_w(3); issue_o(3); VBAR(16); issue_w(4); issue_o(4); VBAR(16);
    } else {
      issue_w(2); issue_w(3);
      wait_flags(ws, fo, tag, SPIN_CAP);
      issue_o(0); issue_o(1);
      VBAR(4); issue_o(2); issue_o(3); VBAR(8); issue_w(4); issue_o(4); VBAR(12);
    }
    issue_w(5); issue_o(5);
    for (int st = 3; st < 13; ++st) { WAITV(16); BARRIER(); issue_w(st + 3); issue_o(st + 3); }
    WAITV(16); BARRIER();
    WAITV(8); BARRIER();
    WAITV(0); BARRIER();
    BARRIER();
    return;
  }
  const int t16 = lane & 15, g = lane >> 4, q = t16 >> 2, p = lane & 3, h = lane >> 5;
  const int wr = wid >> 1, wc = wid & 1;
  unsigned slot = 0, cnt;
  u32x4 qa0, qa1, qb0, qb1;
  const int sw = (t16 >> 1) & 7, fw = (q >> 1) | ((g & 1) << 1);
  const unsigned xl0 = t16 * 128 + ((g ^ sw) << 4), xl1 = t16 * 128 + (((4 + g) ^ sw) << 4);
  const unsigned wl0 = P1_W + (8 * g + q) * 128 + p * 8 + (((wc * 2) ^ fw) << 5), wl1 = wl0 ^ 32;
  unsigned cpf = 0;
  if (wid == 2) {
    const char* pc; asm volatile("s_getpc_b64 %0" : "=s"(pc));
    const char* p = (const char*)((uintptr_t)pc & ~(uintptr_t)127) + (idx * 6 + (lane < 6 ? lane : 0)) * 128;
    if (lane >= 8 && lane < 48) {
      const int c = idx * 40 + lane - 8;
      p = c < 256 ? xA + c * 16384 : c < 640 ? wA + (c - 256) * 16384 : c < 896 ? xB + (c - 640) * 16384 : c < 1024 ? wqB + (c - 896) * 16384 : c < 1152 ? woA + (c - 1024) * 16384 : woB + (c - 1152) * 16384;
    }
    asm volatile("global_load_dword %0, %1, off" : "+{v216}"(cpf) : "v"(p) : "memory");
  }
  GET_TAG();
  {
    f32x16 a0, a1;
    {
      f32x8 ax;
      const unsigned xa0 = lds0 + xl0, xa1 = lds0 + xl1, wa0 = lds0 + wl0 + wr * 8192, wa1 = lds0 + wl1 + wr * 8192;
      const unsigned ea0 = lds0 + P1_W + 16384 + (8 * g + q) * 64 + p * 8 + ((g & 1) << 5), ea1 = ea0 ^ 32, xe = (hg >> 1) * 2048;
#define GB_IN [xa0] "v"(xa0), [xa1] "v"(xa1), [wa0] "v"(wa0), [wa1] "v"(wa1)
      if (wid != 0) asm volatile(GB0_ASM : "=&{v[0:15]}"(a0), "=&{v[16:31]}"(a1), [slot] "+s"(slot), [cnt] "=&s"(cnt) : GB_IN : "memory", "scc", GB0_CLOB);
      else if (hg & 1) asm volatile(GBV_ASM : "=&{v[0:15]}"(a0), "=&{v[16:31]}"(a1), "=&{v[32:39]}"(ax), [slot] "+s"(slot), [cnt] "=&s"(cnt) : GB_IN, [ea0] "v"(ea0), [ea1] "v"(ea1), [xe] "s"(xe) : "memory", "scc", GBV_CLOB);
      else asm volatile(GBK_ASM : "=&{v[0:15]}"(a0), "=&{v[16:31]}"(a1), "=&{v[32:39]}"(ax), [slot] "+s"(slot), [cnt] "=&s"(cnt) : GB_IN, [ea0] "v"(ea0), [ea1] "v"(ea1), [xe] "s"(xe) : "memory", "scc", GBK_CLOB);
      asm volatile("s_waitcnt vmcnt(0)" : "+{v216}"(cpf)::"memory");
      if (wid == 0) {
        const int sub = hg >> 1, tt = 2 * j + (sub >> 1);
        const u32x2 c0 = {cvtpk(ax[0], ax[1]), cvtpk(ax[2], ax[3])}, c1 = {cvtpk(ax[4], ax[5]), cvtpk(ax[6], ax[7])};
        if (hg & 1) {
          const unsigned vo = WS_KVB + tt * 4096 + 2048 + (sub & 1) * 1024 + ((g >> 1) * 32 + t16) * 16 + (g & 1) * 8;
          st8(sws, vo, c0); st8(sws, vo + 256, c1);
        } else st16(sws, WS_KVB + ((tt + 63) & 63) * 4096 + (g >> 1) * 1024 + ((g & 1) * 32 + (sub & 1) * 16 + t16) * 16, u32x4{c0[0], c0[1], c1[0], c1[1]});
      }
    }
    {
      f32x16 c[6];
      c[0] = a0; c[1] = a1;
      const f32x2 qs = {QSCALE, QSCALE};
      asm volatile(GA_ASM
                   : "+{v[0:15]}"(c[0]), "+{v[16:31]}"(c[1]), "=&{v[200:203]}"(qa0), "=&{v[204:207]}"(qa1), "=&{v[208:211]}"(qb0), "=&{v[212:215]}"(qb1), "=&{v[32:47]}"(c[2]), "=&{v[48:63]}"(c[3]), "=&{v[64:79]}"(c[4]), "=&{v[80:95]}"(c[5]), [slot] "+s"(slot), [cnt] "=&s"(cnt)
                   : [xa0] "v"(lds0 + xl0 + wr * 8192), [xa1] "v"(lds0 + xl1 + wr * 8192), [wa0] "v"(lds0 + wl0), [wa1] "v"(lds0 + wl1),
                     [qs] "s"(qs), [pubc] "s"(wid == 0 ? 14 : 99), [zero] "v"(0), [tag] "v"(tag), [fa] "s"(ws + WS_F1 + blockIdx.x * 8)
                   : "memory", "scc", GA_CLOB);
#define CI(mt, nt) c[(6 * (mt) + (nt)) >> 2]
#define CO(mt, nt) (((6 * (mt) + (nt)) & 3) * 4)
      f32x4 kv[2][2] = {}, zz[2] = {};
      const u32x4 ones = {0x3f803f80u, 0x3f803f80u, 0x3f803f80u, 0x3f803f80u};
#pragma unroll
      for (int u = 0; u < 2; ++u) {
        u32x4 pk[2], vv[2];
#pragma unroll
        for (int b = 0; b < 2; ++b) {
          phi4(CI(2 * u, 2 + b), CO(2 * u, 2 + b)); phi4(CI(2 * u + 1, 2 + b), CO(2 * u + 1, 2 + b));
          pk[b] = pack44(CI(2 * u, 2 + b), CO(2 * u, 2 + b), CI(2 * u + 1, 2 + b), CO(2 * u + 1, 2 + b));
          vv[b] = pack44(CI(2 * u, 4 + b), CO(2 * u, 4 + b), CI(2 * u + 1, 4 + b), CO(2 * u + 1, 4 + b));
        }
#pragma unroll
        for (int b = 0; b < 2; ++b) { kv[0][b] = MFMA16(BC8(pk[b]), BC8(vv[0]), kv[0][b]); kv[1][b] = MFMA16(BC8(pk[b]), BC8(vv[1]), kv[1][b]); zz[b] = MFMA16(BC8(pk[b]), BC8(ones), zz[b]); }
      }
#pragma unroll
      for (int mt = 0; mt < 4; ++mt) {
        phi4(CI(mt, 0), CO(mt, 0)); phi4(CI(mt, 1), CO(mt, 1));
        *(LAS u32x4*)(lds + L_PQ + wid * 4096 + (mt >> 1) * 2048 + (g >> 1) * 1024 + ((g & 1) * 32 + (mt & 1) * 16 + t16) * 16) = pack44(CI(mt, 0), CO(mt, 0), CI(mt, 1), CO(mt, 1));
      }
      LAS f32x4* ex = (LAS f32x4*)(lds + L_XCH + wc * 4096 + wr * 2048) + lane;
      ex[0] = kv[0][0]; ex[64] = kv[0][1]; ex[512] = kv[1][0]; ex[576] = kv[1][1];
      if (t16 == 0) { LAS f32x4* ez = (LAS f32x4*)(lds + L_XZ + wc * 128 + wr * 64) + g; ez[0] = zz[0]; ez[16] = zz[1]; }
      BARRIER();
    }
  }
  {
    const unsigned one0 = (lane & 1) ? 0u : 0x3f803f80u, one1 = one0 ^ 0x3f803f80u;
    const u32x4 ones = {one0, one0, one1, one1};
    f32x16 oa, ob; f32x4 la, lb;
    unsigned tmp;
    asm volatile(ATTN_ASM
                 : "=&{v[0:15]}"(oa), "=&{v[16:31]}"(ob), "=&{v[32:35]}"(la), "=&{v[36:39]}"(lb), [cnt] "=&s"(cnt), [tmp] "=&s"(tmp)
                 : [qa0] "v"(qa0), [qa1] "v"(qa1), [qb0] "v"(qb0), [qb1] "v"(qb1), [ones] "v"(ones), [idx] "v"(0xddddddddu),
                   [ka] "v"(lds0 + L_K0 + lane * 16), [lad] "v"(lds0 + lane * 16)
                 : "memory", "scc", ATTN_CLOB);
    const unsigned dst = WS_OB + (j * 64 + hg * 4 + wid) * 2048 + lane * 16;
    attn_finish(oa, la, qa0, qa1, lane, sws, dst);
    attn_finish(ob, lb, qb0, qb1, lane, sws, dst + 65536);
    WAITV(0);
    BARRIER();
  }
  {
    f32x16 c[4];
    const int f = 2 * (g & 1) + (q >> 1);
    const unsigned wb = lds0 + 16384 + wc * 8192 + (16 * (g >> 1) + 4 * (g & 1) + q) * 128 + p * 8 + (f << 5);
    slot = 0;
    asm volatile(GO_ASM
                 : "=&{v[0:15]}"(c[0]), "=&{v[16:31]}"(c[1]), "=&{v[32:47]}"(c[2]), "=&{v[48:63]}"(c[3]), [slot] "+s"(slot), [cnt] "=&s"(cnt)
                 : [xa0] "v"(lds0 + wr * 8192 + (g >> 1) * 1024 + ((g & 1) * 32 + t16) * 16), [wa0] "v"(wb), [wa1] "v"(wb ^ 32), [wa2] "v"(wb ^ 64), [wa3] "v"(wb ^ 96)
                 : "memory", "scc", GO_CLOB);
    const srd_t so = mk_srd(out, 8u << 20);
    lds_ptr st = lds + wid * 9216;
    const unsigned o = (ro * 128 + wr * 64 + (lane >> 3)) * 4096 + br * 2048 + lc * 256 + wc * 128 + (lane & 7) * 16;
#pragma unroll
    for (int mt = 0; mt < 5; ++mt) {
      u32x4 r0, r1;
      if (mt > 0) { r0 = *(LAS u32x4*)(st + (mt * 16 - 16 + (lane >> 3)) * 144 + (lane & 7) * 16); r1 = *(LAS u32x4*)(st + (mt * 16 - 8 + (lane >> 3)) * 144 + (lane & 7) * 16); }
      if (mt < 4) {
#pragma unroll
        for (int n = 0; n < 4; ++n) *(LAS u32x2*)(st + (mt * 16 + t16) * 144 + n * 32 + g * 8) = u32x2{cvtpk(c[mt][4 * n], c[mt][4 * n + 1]), cvtpk(c[mt][4 * n + 2], c[mt][4 * n + 3])};
      }
      if (mt > 0) { __builtin_amdgcn_raw_buffer_store_b128(r0, so, o, (mt - 1) * 65536, 16); __builtin_amdgcn_raw_buffer_store_b128(r1, so, o, (mt - 1) * 65536 + 32768, 16); }
      __builtin_amdgcn_sched_barrier(0);
    }
  }
}

extern "C" void kernel_launch(void* const* d_in, const int* in_sizes, int n_in, void* d_out, int out_size, void* d_ws, size_t ws_size, hipStream_t stream) {
  (void)hipFuncSetAttribute((const void*)fused, hipFuncAttributeMaxDynamicSharedMemorySize, LDS_BYTES);
  fused<<<256, 512, LDS_BYTES, stream>>>((const char*)d_in[0], (const char*)d_in[1], (const char*)d_in[2], (const char*)d_in[3], (const char*)d_in[4], (const char*)d_in[5],
                                         (const char*)d_in[6], (const char*)d_in[7], (char*)d_out, (char*)d_ws + ((ws_size - (16u << 20)) & ~(size_t)0xfffff));
}
```
